```python
import torch

def _softmax(x, axis=-1):
    return torch.softmax(x, dim=axis)

def _gelu(x):
    return torch.nn.functional.gelu(x, approximate='tanh')

def _silu(x):
    return torch.nn.functional.silu(x)

def _sigmoid(x):
    return torch.sigmoid(x)

def _layernorm(x, axis=-1, eps=1e-05):
    m = x.mean(dim=axis, keepdim=True)
    v = x.var(dim=axis, keepdim=True, unbiased=False)
    return (x - m) / torch.sqrt(v + eps)

def _scatter_add(tgt, idx, src, axis=0):
    return tgt.clone().index_add_(axis, idx, src)

def _segment_sum(src, seg, n):
    out = torch.zeros((n,) + src.shape[1:], dtype=src.dtype)
    return out.index_add_(0, seg, src)

def _pad(x, pw):
    flat = tuple((v for pair in reversed(pw) for v in pair))
    return torch.nn.functional.pad(x, flat)
kernel_name = 'sequential_preLN__memory_aug_recurrent_mem_tr__glu_gated_reglu__prompt'

def setup_inputs():

    _w = lambda a: (lambda t: t.to(torch.bfloat16) if t.is_floating_point() else t)(torch.as_tensor(a))

    def _A_setup():
        B, S, Dm = (1, 2048, 448)
        H, Dh = (16, 256)
        M, seg = (8, 32)
        seg = min(seg, S)
        S = S // seg * seg
        return {'x': _w((lambda *s: torch.randn(*s, dtype=torch.bfloat16) * 0.05)(B, S, Dm)), 'mem0': _w((lambda *s: torch.randn(*s, dtype=torch.bfloat16) * 0.2)(B, M, Dm)), 'Wq': _w((lambda *s: torch.randn(*s, dtype=torch.bfloat16) * 1.6)(Dm, H, Dh) * Dm ** (-0.5)), 'Wk': _w((lambda *s: torch.randn(*s, dtype=torch.bfloat16) * 1.6)(Dm, H, Dh) * Dm ** (-0.5)), 'Wv': _w((lambda *s: torch.randn(*s, dtype=torch.bfloat16) * 0.2)(Dm, H, Dh) * Dm ** (-0.5)), 'Wo': _w((lambda *s: torch.randn(*s, dtype=torch.bfloat16) * 0.2)(H, Dh, Dm) * (H * Dh) ** (-0.5)), 'seg_len': seg}
    _A_in = _A_setup()

    def _B_setup():
        B, S, D, F = (1, 2048, 448, 1792)
        return {'x': _w((lambda *s: torch.randn(*s, dtype=torch.bfloat16) * 0.05)(B, S, D)), 'W_gate': _w((lambda *s: torch.randn(*s, dtype=torch.bfloat16) * 0.2)(D, F) * (2.0 / D) ** 0.5), 'W_up': _w((lambda *s: torch.randn(*s, dtype=torch.bfloat16) * 0.2)(D, F) * (2.0 / D) ** 0.5), 'W_down': _w((lambda *s: torch.randn(*s, dtype=torch.bfloat16) * 0.2)(F, D) * (1.0 / F) ** 0.5)}
    _B_in = _B_setup()
    return {'A_x': _A_in['x'], 'A_mem0': _A_in['mem0'], 'A_Wq': _A_in['Wq'], 'A_Wk': _A_in['Wk'], 'A_Wv': _A_in['Wv'], 'A_Wo': _A_in['Wo'], 'A_seg_len': _A_in['seg_len'], 'B_x': _B_in['x'], 'B_W_gate': _B_in['W_gate'], 'B_W_up': _B_in['W_up'], 'B_W_down': _B_in['W_down']}

def reference(A_x, A_mem0, A_Wq, A_Wk, A_Wv, A_Wo, A_seg_len, B_x, B_W_gate, B_W_up, B_W_down):

    def _A_ref(x, mem0, Wq, Wk, Wv, Wo, seg_len):
        B, S, Dm = x.shape
        H, Dh = (Wq.shape[1], Wq.shape[2])
        M = mem0.shape[1]
        scale = Dh ** (-0.5)
        n_seg = S // seg_len
        L = M + seg_len
        idx = torch.arange(L)
        mask = (idx[:, None] < M) | (idx[:, None] >= idx[None, :])
        mem = mem0
        outs = []
        for s in range(n_seg):
            xs = x[:, s * seg_len:(s + 1) * seg_len, :].clone()
            h = torch.concatenate([mem, xs], axis=1)
            q = torch.einsum('btd,dhe->bhte', h, Wq)
            k = torch.einsum('btd,dhe->bhte', h, Wk)
            v = torch.einsum('btd,dhe->bhte', h, Wv)
            scores = torch.einsum('bhqe,bhke->bhqk', q, k) * scale
            scores = torch.where(mask[None, None], scores, -1000000000.0)
            p = _softmax(scores, axis=-1)
            o = torch.einsum('bhqk,bhke->bhqe', p, v)
            o = torch.einsum('bhte,hed->btd', o, Wo)
            mem = o[:, :M, :].clone()
            outs.append(o[:, M:, :])
        return torch.concatenate(outs, axis=1)

    def _B_ref(x, W_gate, W_up, W_down):
        gate = torch.einsum('bsd,df->bsf', x, W_gate)
        up = torch.einsum('bsd,df->bsf', x, W_up)
        gate = gate * (lambda x, dt: x.to(dt))(gate > 0, gate.dtype)
        h = gate * up
        return torch.einsum('bsf,fd->bsd', h, W_down)
    x = A_x
    h = x + _A_ref(_layernorm(x, axis=-1), A_mem0, A_Wq, A_Wk, A_Wv, A_Wo, A_seg_len)
    h2 = h + _B_ref(_layernorm(h, axis=-1), B_W_gate, B_W_up, B_W_down)
    return h2
```

```cpp
#define NB 256
#include <hip/hip_runtime.h>
#include <stdint.h>

typedef __bf16 bf16_t;
typedef bf16_t bf16x2 __attribute__((ext_vector_type(2)));
typedef bf16_t bf16x4 __attribute__((ext_vector_type(4)));
typedef bf16_t bf16x8 __attribute__((ext_vector_type(8)));
typedef bf16_t bf16x16 __attribute__((ext_vector_type(16)));
typedef float f32x2 __attribute__((ext_vector_type(2)));
typedef float f32x4 __attribute__((ext_vector_type(4)));
typedef unsigned u32x2 __attribute__((ext_vector_type(2)));
typedef unsigned u32x4 __attribute__((ext_vector_type(4)));
typedef unsigned u32x8 __attribute__((ext_vector_type(8)));
typedef short s16x4 __attribute__((ext_vector_type(4)));
typedef unsigned short u16x2 __attribute__((ext_vector_type(2)));
#define DI __device__ __forceinline__
#define GAS __attribute__((address_space(1)))
#define LAS __attribute__((address_space(3)))
#define RLX_AGENT __ATOMIC_RELAXED, __HIP_MEMORY_SCOPE_AGENT
typedef GAS unsigned gu32;
typedef GAS unsigned long long gu64;

constexpr int DM = 448, NH = 16, DH = 256, SEQ = 2048, NSEG = 64, FF = 1792;
constexpr size_t OFF_XF = 0;
constexpr size_t OFF_AF = 2u << 20;
constexpr size_t OFF_BF = 9u << 20;
constexpr size_t OFF_ACC = 64u << 20;
constexpr size_t OFF_FLG = 17u << 20;
constexpr size_t OFF_STAT = OFF_FLG + 262144;
constexpr size_t OFF_REC = 18u << 20;
constexpr size_t OFF_PO = 34u << 20;
constexpr int MATF = 28 * 14 * 1024;
constexpr int REC_BYTES = 14336;

DI unsigned pk2(float a, float b) { f32x2 v = {a, b}; return __builtin_bit_cast(unsigned, __builtin_convertvector(v, bf16x2)); }
DI float bflo(unsigned u) { return __builtin_bit_cast(float, u << 16); }
DI float bfhi(unsigned u) { return __builtin_bit_cast(float, u & 0xffff0000u); }
DI float rbf(float a) { return bflo(pk2(a, a)); }
DI f32x4 mfma32(bf16x8 a, bf16x8 b, f32x4 c) { return __builtin_amdgcn_mfma_f32_16x16x32_bf16(a, b, c, 0, 0, 0); }
DI f32x4 smfma(bf16x8 a, bf16x16 b, f32x4 c, int idx) { return __builtin_amdgcn_smfmac_f32_16x16x64_bf16(a, b, c, idx, 0, 0); }
DI void lds_barrier() { asm volatile("s_waitcnt lgkmcnt(0)\n\ts_barrier" ::: "memory"); }
template <typename T> DI T lds_ld(unsigned a) { return *(LAS T*)(uintptr_t)a; }
template <typename T> DI void lds_st(unsigned a, T v) { *(LAS T*)(uintptr_t)a = v; }
DI u32x2 lds_tr(unsigned a) { return __builtin_bit_cast(u32x2, __builtin_amdgcn_ds_read_tr16_b64_v4i16((LAS s16x4*)(uintptr_t)a)); }
DI float xor16_max(float v) { auto r = __builtin_amdgcn_permlane16_swap(__float_as_uint(v), __float_as_uint(v), false, false); return fmaxf(__uint_as_float(r[0]), __uint_as_float(r[1])); }
DI float xor32_max(float v) { auto r = __builtin_amdgcn_permlane32_swap(__float_as_uint(v), __float_as_uint(v), false, false); return fmaxf(__uint_as_float(r[0]), __uint_as_float(r[1])); }
DI float xor16_add(float v) { auto r = __builtin_amdgcn_permlane16_swap(__float_as_uint(v), __float_as_uint(v), false, false); return __uint_as_float(r[0]) + __uint_as_float(r[1]); }
DI float xor32_add(float v) { auto r = __builtin_amdgcn_permlane32_swap(__float_as_uint(v), __float_as_uint(v), false, false); return __uint_as_float(r[0]) + __uint_as_float(r[1]); }
DI float ror8(float v) { return __builtin_bit_cast(float, __builtin_amdgcn_update_dpp(0, __builtin_bit_cast(int, v), 0x128, 0xf, 0xf, false)); }
DI int colA(int T, int n16) {
  const int w = T / 7, tau = T - 7 * w;
  const int beta = tau < 6 ? 3 * w + (tau >> 1) : 12 + (w >> 1);
  const int par = tau < 6 ? (tau & 1) : (w & 1);
  return 32 * beta + 8 * (n16 >> 2) + 4 * ((n16 >> 1) & 1) + 2 * par + (n16 & 1);
}
#define LS 128
#define ACCINIT 0x80000000u
__global__ void __launch_bounds__(512) k_prologue(const bf16_t* __restrict__ x, const bf16_t* __restrict__ Wq, const bf16_t* __restrict__ Wk,
                                                  const bf16_t* __restrict__ Wv, const bf16_t* __restrict__ Wo, unsigned char* __restrict__ ws) {
  __shared__ __attribute__((aligned(16))) unsigned char sm[256 * 288 + 4096];
  const int tid = threadIdx.x, lane = tid & 63, w = __builtin_amdgcn_readfirstlane(tid >> 6);
  const int b = (blockIdx.x & 7) * 32 + (blockIdx.x >> 3);
  const int n16 = lane & 15, g = lane >> 4;
  const int mi = b >> 3, hf = (b >> 2) & 1, cg = b & 3, h = mi & 15;
  const bool isB = mi >= 16;
  const unsigned sb = (unsigned)(uintptr_t)(LAS unsigned char*)sm;
  const int kc = 7 * hf + w;
  bf16x8 fa[8], fb[8];
  u32x4 v[14];
  if (w < 7) {
    const bf16_t* Wrow = isB ? Wv : Wq;
    const int ra = 32 * kc + 8 * (n16 >> 2) + (n16 & 3);
#pragma unroll
    for (int ks = 0; ks < 8; ++ks) {
      fa[ks] = *(const bf16x8*)(Wrow + ((size_t)ra * NH + h) * DH + ks * 32 + g * 8);
      fb[ks] = *(const bf16x8*)(Wrow + ((size_t)(ra + 4) * NH + h) * DH + ks * 32 + g * 8);
    }
  } else if (b < 128) {
    const bf16_t* xr = x + (size_t)(b * 16 + n16) * DM + g * 8;
#pragma unroll
    for (int k = 0; k < 14; ++k) v[k] = *(const u32x4*)(xr + k * 32);
  }
  {
    u32x4 c[7];
    if (!isB) {
#pragma unroll
      for (int t = 0; t < 7; ++t) c[t] = *(const u32x4*)(Wk + ((size_t)colA(7 * cg + t, n16) * NH + h) * DH + w * 32 + g * 8);
#pragma unroll
      for (int t = 0; t < 7; ++t) lds_st<u32x4>(sb + (t * 8 + w) * 1024 + lane * 16, c[t]);
    } else {
#pragma unroll
      for (int i = 0; i < 7; ++i) { const int p = tid + i * 512, e = p / 14, q = p - 14 * e; c[i] = *(const u32x4*)(Wo + ((size_t)h * DH + e) * DM + cg * 112 + q * 8); }
#pragma unroll
      for (int i = 0; i < 7; ++i) { const int p = tid + i * 512, e = p / 14, q = p - 14 * e; lds_st<u32x4>(sb + e * 288 + q * 16, c[i]); }
    }
  }
  __syncthreads();
  float* red = (float*)(sm + 256 * 288);
  if (w < 7) {
    unsigned char* dst = ws + (isB ? OFF_BF : OFF_AF) + (size_t)h * MATF;
    const int lpos = isB ? (g * 16 + 8 * ((n16 >> 1) & 1) + 2 * (n16 >> 2) + (n16 & 1)) : lane;
    const float sc = isB ? 1.0f : 0.0625f;
#pragma unroll
    for (int t = 0; t < 7; ++t) {
      bf16x8 cf[8];
      if (!isB) {
#pragma unroll
        for (int ks = 0; ks < 8; ++ks) cf[ks] = lds_ld<bf16x8>(sb + (t * 8 + ks) * 1024 + lane * 16);
      } else {
#pragma unroll
        for (int ks = 0; ks < 8; ++ks) {
          const unsigned a = sb + (ks * 32 + g * 8 + (n16 >> 2)) * 288 + t * 32 + (n16 & 3) * 8;
          const u32x2 lo = lds_tr(a), hi = lds_tr(a + 4 * 288);
          cf[ks] = __builtin_bit_cast(bf16x8, u32x4{lo[0], lo[1], hi[0], hi[1]});
        }
      }
      f32x4 ca = {0, 0, 0, 0}, cb = {0, 0, 0, 0};
#pragma unroll
      for (int ks = 0; ks < 8; ++ks) { ca = mfma32(fa[ks], cf[ks], ca); cb = mfma32(fb[ks], cf[ks], cb); }
      const u32x4 o = {pk2(ca[0] * sc, ca[1] * sc), pk2(ca[2] * sc, ca[3] * sc), pk2(cb[0] * sc, cb[1] * sc), pk2(cb[2] * sc, cb[3] * sc)};
      *(u32x4*)(dst + ((size_t)((7 * cg + t) * 14 + kc) * 64 + lpos) * 16) = o;
      if (isB) {
        float nrm = 0.f;
#pragma unroll
        for (int i = 0; i < 4; ++i) { const float a = bflo(o[i]), c = bfhi(o[i]); nrm += a * a + c * c; }
        nrm = xor32_add(xor16_add(nrm));
        if (g == 0) red[w * 128 + t * 16 + n16] = nrm;
      }
    }
  } else if (b >= 128) {
    const int j = b - 128;
    const u32x4 z = {0, 0, 0, 0}, zi = {ACCINIT, ACCINIT, ACCINIT, ACCINIT};
    const __amdgpu_buffer_rsrc_t rz = __builtin_amdgcn_make_buffer_rsrc(ws + OFF_ACC, 0, 64 * 112 * LS, 0x00020000);
    const __amdgpu_buffer_rsrc_t rf = __builtin_amdgcn_make_buffer_rsrc(ws + OFF_FLG, 0, 262144, 0x00020000);
#pragma unroll
    for (int i = 0; i < 7; ++i) __builtin_amdgcn_raw_buffer_store_b128(zi, rz, ((j * 7 + i) * 8 + (lane >> 3)) * LS + (lane & 7) * 16, 0, 16);
    __builtin_amdgcn_raw_buffer_store_b128(z, rf, j * 1024 + lane * 16, 0, 16);
    __builtin_amdgcn_raw_buffer_store_b128(z, rf, 131072 + j * 1024 + lane * 16, 0, 16);
  } else {
    float s = 0.f;
#pragma unroll
    for (int k = 0; k < 14; ++k)
#pragma unroll
      for (int i = 0; i < 4; ++i) s += bflo(v[k][i]) + bfhi(v[k][i]);
    s = xor32_add(xor16_add(s));
    const float mean = s * (1.0f / DM);
    float q = 0.f;
#pragma unroll
    for (int k = 0; k < 14; ++k)
#pragma unroll
      for (int i = 0; i < 4; ++i) { float a = bflo(v[k][i]) - mean, c = bfhi(v[k][i]) - mean; q += a * a + c * c; }
    q = xor32_add(xor16_add(q));
    const float mb = rbf(mean), vb = rbf(q * (1.0f / DM));
    const float rd = 1.0f / rbf(sqrtf(rbf(vb + 1e-5f)));
    float nn = 0.f;
#pragma unroll
    for (int k = 0; k < 14; ++k) {
      u32x4 o;
#pragma unroll
      for (int i = 0; i < 4; ++i) {
        const unsigned r = pk2(bflo(v[k][i]) - mb, bfhi(v[k][i]) - mb);
        o[i] = pk2(bflo(r) * rd, bfhi(r) * rd);
        float a = bflo(o[i]), c = bfhi(o[i]); nn += a * a + c * c;
      }
      *(u32x4*)(ws + OFF_XF + ((size_t)(b * 14 + k) * 64 + lane) * 16) = o;
    }
    nn = xor32_add(xor16_add(nn));
#pragma unroll
    for (int o = 1; o < 16; o <<= 1) nn = fmaxf(nn, __shfl_xor(nn, o));
    if (lane == 0) ((float*)(ws + OFF_STAT))[128 + b] = nn;
  }
  if (isB) {
    __syncthreads();
    if (w == 0) {
      float m = 0.f;
#pragma unroll
      for (int r = 0; r < 2; ++r) {
        const int c = r * 64 + lane < 112 ? r * 64 + lane : 0;
        float s = 0.f;
#pragma unroll
        for (int q = 0; q < 7; ++q) s += red[q * 128 + c];
        m = fmaxf(m, s);
      }
#pragma unroll
      for (int o = 1; o < 64; o <<= 1) m = fmaxf(m, __shfl_xor(m, o));
      if (lane == 0) ((float*)(ws + OFF_STAT))[hf * 64 + h * 4 + cg] = m;
    }
  }
}
constexpr unsigned L_XS = 0, L_SPX = 28672, L_XS1 = 32768, L_SPM = 61440, L_DUM = 62464, L_MX = 63488, L_MI = 65536, L_TB = 73728, L_BS = 81920, L_CH_END = 147456;
typedef unsigned u32x32 __attribute__((ext_vector_type(32)));
template <int N> DI void dma_kb(const unsigned char* base, unsigned voff, unsigned m0v) {
#pragma unroll
  for (int i = 0; i < N; ++i) {
    unsigned sv;
    asm volatile("s_mov_b32 %0, m0\n\ts_mov_b32 m0, %3\n\ts_nop 3\n\tglobal_load_lds_dwordx4 %1, %2\n\ts_mov_b32 m0, %0"
                 : "=&s"(sv) : "v"(voff + i * 1024u), "s"(base), "s"(m0v + i * 1024u) : "memory");
  }
}
DI void chain_role(const int h, const int c, const bf16_t* __restrict__ mem0, unsigned char* ws) {
  const int tid = threadIdx.x, lane = tid & 63, w = __builtin_amdgcn_readfirstlane(tid >> 6);
  const int n16 = lane & 15, g = lane >> 4;
  const unsigned char* bsrc = ws + OFF_BF + (size_t)h * MATF + (size_t)(4 * c + w) * 14336;
  const unsigned char* asrc = ws + OFF_AF + (size_t)h * MATF + (size_t)w * 7 * 14336;
#pragma unroll
  for (int j = 2; j < 7; ++j) {
    const int kk = 2 * w + j < 7 ? 2 * w + j : 2 * w + j - 7;
    dma_kb<2>(bsrc, lane * 16 + kk * 2048, L_BS + w * 16384 + (j - 2) * 2048);
    if (j >= 4) dma_kb<2>(asrc, lane * 16 + kk * 2048, L_BS + w * 16384 + 10240 + (j - 4) * 2048);
  }
  dma_kb<7>(ws + OFF_XF, lane * 16 + w * 7168, L_XS + w * 7168);
  u32x32 ga[8], gv[4];
  u32x4 a, b2;
  float lam, la2, rx, rx2;
  unsigned dm;
  asm volatile(
  "global_load_dwordx4 %[ma], %[mp], off\n"
  "global_load_dwordx4 %[mb], %[mp], off offset:16\n"
  "s_add_u32 s88, %[wl], 0x1140000\n"
  "s_addc_u32 s89, %[wh], 0\n"
  "global_load_dword %[st0], %[l4], s[88:89] offset:0\n"
  "global_load_dword %[st1], %[l4], s[88:89] offset:256\n"
  "global_load_dword %[st2], %[l4], s[88:89] offset:512\n"
  "global_load_dword %[st3], %[l4], s[88:89] offset:768\n"
  "s_mul_i32 s93, %[h], 401408\n"
  "s_mul_i32 s94, %[w], 100352\n"
  "s_add_u32 s94, s94, s93\n"
  "s_add_u32 s94, s94, 0x200000\n"
  "s_lshl_b32 s95, %[w], 1\n"
  "s_lshl_b32 s96, s95, 11\n"
  "s_add_u32 s96, s96, s94\n"
  "s_add_u32 s88, %[wl], s96\n"
  "s_addc_u32 s89, %[wh], 0\n"
  "global_load_dwordx4 a[0:3], %[l16], s[88:89] offset:0\n"
  "global_load_dwordx4 a[4:7], %[l16], s[88:89] offset:1024\n"
  "s_add_u32 s88, s88, 14336\n"
  "s_addc_u32 s89, s89, 0\n"
  "global_load_dwordx4 a[32:35], %[l16], s[88:89] offset:0\n"
  "global_load_dwordx4 a[36:39], %[l16], s[88:89] offset:1024\n"
  "s_add_u32 s88, s88, 14336\n"
  "s_addc_u32 s89, s89, 0\n"
  "global_load_dwordx4 a[88:91], %[l16], s[88:89] offset:0\n"
  "global_load_dwordx4 a[92:95], %[l16], s[88:89] offset:1024\n"
  "s_add_u32 s88, s88, 14336\n"
  "s_addc_u32 s89, s89, 0\n"
  "global_load_dwordx4 a[144:147], %[l16], s[88:89] offset:0\n"
  "global_load_dwordx4 a[148:151], %[l16], s[88:89] offset:1024\n"
  "s_add_u32 s88, s88, 14336\n"
  "s_addc_u32 s89, s89, 0\n"
  "global_load_dwordx4 a[200:203], %[l16], s[88:89] offset:0\n"
  "global_load_dwordx4 a[204:207], %[l16], s[88:89] offset:1024\n"
  "s_add_u32 s88, s88, 14336\n"
  "s_addc_u32 s89, s89, 0\n"
  "global_load_dwordx4 v[144:147], %[l16], s[88:89] offset:0\n"
  "global_load_dwordx4 v[148:151], %[l16], s[88:89] offset:1024\n"
  "s_add_u32 s88, s88, 14336\n"
  "s_addc_u32 s89, s89, 0\n"
  "global_load_dwordx4 v[200:203], %[l16], s[88:89] offset:0\n"
  "global_load_dwordx4 v[204:207], %[l16], s[88:89] offset:1024\n"
  "s_add_u32 s95, s95, 1\n"
  "s_cmp_eq_u32 s95, 7\n"
  "s_cselect_b32 s95, 0, s95\n"
  "s_lshl_b32 s96, s95, 11\n"
  "s_add_u32 s96, s96, s94\n"
  "s_add_u32 s88, %[wl], s96\n"
  "s_addc_u32 s89, %[wh], 0\n"
  "global_load_dwordx4 a[8:11], %[l16], s[88:89] offset:0\n"
  "global_load_dwordx4 a[12:15], %[l16], s[88:89] offset:1024\n"
  "s_add_u32 s88, s88, 14336\n"
  "s_addc_u32 s89, s89, 0\n"
  "global_load_dwordx4 a[40:43], %[l16], s[88:89] offset:0\n"
  "global_load_dwordx4 a[44:47], %[l16], s[88:89] offset:1024\n"
  "s_add_u32 s88, s88, 14336\n"
  "s_addc_u32 s89, s89, 0\n"
  "global_load_dwordx4 a[96:99], %[l16], s[88:89] offset:0\n"
  "global_load_dwordx4 a[100:103], %[l16], s[88:89] offset:1024\n"
  "s_add_u32 s88, s88, 14336\n"
  "s_addc_u32 s89, s89, 0\n"
  "global_load_dwordx4 a[152:155], %[l16], s[88:89] offset:0\n"
  "global_load_dwordx4 a[156:159], %[l16], s[88:89] offset:1024\n"
  "s_add_u32 s88, s88, 14336\n"
  "s_addc_u32 s89, s89, 0\n"
  "global_load_dwordx4 a[208:211], %[l16], s[88:89] offset:0\n"
  "global_load_dwordx4 a[212:215], %[l16], s[88:89] offset:1024\n"
  "s_add_u32 s88, s88, 14336\n"
  "s_addc_u32 s89, s89, 0\n"
  "global_load_dwordx4 v[152:155], %[l16], s[88:89] offset:0\n"
  "global_load_dwordx4 v[156:159], %[l16], s[88:89] offset:1024\n"
  "s_add_u32 s88, s88, 14336\n"
  "s_addc_u32 s89, s89, 0\n"
  "global_load_dwordx4 v[208:211], %[l16], s[88:89] offset:0\n"
  "global_load_dwordx4 v[212:215], %[l16], s[88:89] offset:1024\n"
  "s_add_u32 s95, s95, 1\n"
  "s_cmp_eq_u32 s95, 7\n"
  "s_cselect_b32 s95, 0, s95\n"
  "s_lshl_b32 s96, s95, 11\n"
  "s_add_u32 s96, s96, s94\n"
  "s_add_u32 s88, %[wl], s96\n"
  "s_addc_u32 s89, %[wh], 0\n"
  "global_load_dwordx4 a[16:19], %[l16], s[88:89] offset:0\n"
  "global_load_dwordx4 a[20:23], %[l16], s[88:89] offset:1024\n"
  "s_add_u32 s88, s88, 14336\n"
  "s_addc_u32 s89, s89, 0\n"
  "global_load_dwordx4 a[48:51], %[l16], s[88:89] offset:0\n"
  "global_load_dwordx4 a[52:55], %[l16], s[88:89] offset:1024\n"
  "s_add_u32 s88, s88, 14336\n"
  "s_addc_u32 s89, s89, 0\n"
  "global_load_dwordx4 a[104:107], %[l16], s[88:89] offset:0\n"
  "global_load_dwordx4 a[108:111], %[l16], s[88:89] offset:1024\n"
  "s_add_u32 s88, s88, 14336\n"
  "s_addc_u32 s89, s89, 0\n"
  "global_load_dwordx4 a[160:163], %[l16], s[88:89] offset:0\n"
  "global_load_dwordx4 a[164:167], %[l16], s[88:89] offset:1024\n"
  "s_add_u32 s88, s88, 14336\n"
  "s_addc_u32 s89, s89, 0\n"
  "global_load_dwordx4 a[216:219], %[l16], s[88:89] offset:0\n"
  "global_load_dwordx4 a[220:223], %[l16], s[88:89] offset:1024\n"
  "s_add_u32 s88, s88, 14336\n"
  "s_addc_u32 s89, s89, 0\n"
  "global_load_dwordx4 v[160:163], %[l16], s[88:89] offset:0\n"
  "global_load_dwordx4 v[164:167], %[l16], s[88:89] offset:1024\n"
  "s_add_u32 s88, s88, 14336\n"
  "s_addc_u32 s89, s89, 0\n"
  "global_load_dwordx4 v[216:219], %[l16], s[88:89] offset:0\n"
  "global_load_dwordx4 v[220:223], %[l16], s[88:89] offset:1024\n"
  "s_add_u32 s95, s95, 1\n"
  "s_cmp_eq_u32 s95, 7\n"
  "s_cselect_b32 s95, 0, s95\n"
  "s_lshl_b32 s96, s95, 11\n"
  "s_add_u32 s96, s96, s94\n"
  "s_add_u32 s88, %[wl], s96\n"
  "s_addc_u32 s89, %[wh], 0\n"
  "global_load_dwordx4 a[24:27], %[l16], s[88:89] offset:0\n"
  "global_load_dwordx4 a[28:31], %[l16], s[88:89] offset:1024\n"
  "s_add_u32 s88, s88, 14336\n"
  "s_addc_u32 s89, s89, 0\n"
  "global_load_dwordx4 a[56:59], %[l16], s[88:89] offset:0\n"
  "global_load_dwordx4 a[60:63], %[l16], s[88:89] offset:1024\n"
  "s_add_u32 s88, s88, 14336\n"
  "s_addc_u32 s89, s89, 0\n"
  "global_load_dwordx4 a[112:115], %[l16], s[88:89] offset:0\n"
  "global_load_dwordx4 a[116:119], %[l16], s[88:89] offset:1024\n"
  "s_add_u32 s88, s88, 14336\n"
  "s_addc_u32 s89, s89, 0\n"
  "global_load_dwordx4 a[168:171], %[l16], s[88:89] offset:0\n"
  "global_load_dwordx4 a[172:175], %[l16], s[88:89] offset:1024\n"
  "s_add_u32 s88, s88, 14336\n"
  "s_addc_u32 s89, s89, 0\n"
  "global_load_dwordx4 a[224:227], %[l16], s[88:89] offset:0\n"
  "global_load_dwordx4 a[228:231], %[l16], s[88:89] offset:1024\n"
  "s_add_u32 s88, s88, 14336\n"
  "s_addc_u32 s89, s89, 0\n"
  "global_load_dwordx4 v[168:171], %[l16], s[88:89] offset:0\n"
  "global_load_dwordx4 v[172:175], %[l16], s[88:89] offset:1024\n"
  "s_add_u32 s88, s88, 14336\n"
  "s_addc_u32 s89, s89, 0\n"
  "global_load_dwordx4 v[224:227], %[l16], s[88:89] offset:0\n"
  "global_load_dwordx4 v[228:231], %[l16], s[88:89] offset:1024\n"
  "s_add_u32 s95, s95, 1\n"
  "s_cmp_eq_u32 s95, 7\n"
  "s_cselect_b32 s95, 0, s95\n"
  "s_lshl_b32 s96, s95, 11\n"
  "s_add_u32 s96, s96, s94\n"
  "s_add_u32 s88, %[wl], s96\n"
  "s_addc_u32 s89, %[wh], 0\n"
  "s_add_u32 s88, s88, 14336\n"
  "s_addc_u32 s89, s89, 0\n"
  "global_load_dwordx4 a[64:67], %[l16], s[88:89] offset:0\n"
  "global_load_dwordx4 a[68:71], %[l16], s[88:89] offset:1024\n"
  "s_add_u32 s88, s88, 14336\n"
  "s_addc_u32 s89, s89, 0\n"
  "global_load_dwordx4 a[120:123], %[l16], s[88:89] offset:0\n"
  "global_load_dwordx4 a[124:127], %[l16], s[88:89] offset:1024\n"
  "s_add_u32 s88, s88, 14336\n"
  "s_addc_u32 s89, s89, 0\n"
  "global_load_dwordx4 a[176:179], %[l16], s[88:89] offset:0\n"
  "global_load_dwordx4 a[180:183], %[l16], s[88:89] offset:1024\n"
  "s_add_u32 s88, s88, 14336\n"
  "s_addc_u32 s89, s89, 0\n"
  "global_load_dwordx4 a[232:235], %[l16], s[88:89] offset:0\n"
  "global_load_dwordx4 a[236:239], %[l16], s[88:89] offset:1024\n"
  "s_add_u32 s88, s88, 14336\n"
  "s_addc_u32 s89, s89, 0\n"
  "global_load_dwordx4 v[176:179], %[l16], s[88:89] offset:0\n"
  "global_load_dwordx4 v[180:183], %[l16], s[88:89] offset:1024\n"
  "s_add_u32 s88, s88, 14336\n"
  "s_addc_u32 s89, s89, 0\n"
  "global_load_dwordx4 v[232:235], %[l16], s[88:89] offset:0\n"
  "global_load_dwordx4 v[236:239], %[l16], s[88:89] offset:1024\n"
  "s_add_u32 s95, s95, 1\n"
  "s_cmp_eq_u32 s95, 7\n"
  "s_cselect_b32 s95, 0, s95\n"
  "s_lshl_b32 s96, s95, 11\n"
  "s_add_u32 s96, s96, s94\n"
  "s_add_u32 s88, %[wl], s96\n"
  "s_addc_u32 s89, %[wh], 0\n"
  "s_add_u32 s88, s88, 14336\n"
  "s_addc_u32 s89, s89, 0\n"
  "global_load_dwordx4 a[72:75], %[l16], s[88:89] offset:0\n"
  "global_load_dwordx4 a[76:79], %[l16], s[88:89] offset:1024\n"
  "s_add_u32 s88, s88, 14336\n"
  "s_addc_u32 s89, s89, 0\n"
  "global_load_dwordx4 a[128:131], %[l16], s[88:89] offset:0\n"
  "global_load_dwordx4 a[132:135], %[l16], s[88:89] offset:1024\n"
  "s_add_u32 s88, s88, 14336\n"
  "s_addc_u32 s89, s89, 0\n"
  "global_load_dwordx4 a[184:187], %[l16], s[88:89] offset:0\n"
  "global_load_dwordx4 a[188:191], %[l16], s[88:89] offset:1024\n"
  "s_add_u32 s88, s88, 14336\n"
  "s_addc_u32 s89, s89, 0\n"
  "global_load_dwordx4 a[240:243], %[l16], s[88:89] offset:0\n"
  "global_load_dwordx4 a[244:247], %[l16], s[88:89] offset:1024\n"
  "s_add_u32 s88, s88, 14336\n"
  "s_addc_u32 s89, s89, 0\n"
  "global_load_dwordx4 v[184:187], %[l16], s[88:89] offset:0\n"
  "global_load_dwordx4 v[188:191], %[l16], s[88:89] offset:1024\n"
  "s_add_u32 s88, s88, 14336\n"
  "s_addc_u32 s89, s89, 0\n"
  "global_load_dwordx4 v[240:243], %[l16], s[88:89] offset:0\n"
  "global_load_dwordx4 v[244:247], %[l16], s[88:89] offset:1024\n"
  "s_add_u32 s95, s95, 1\n"
  "s_cmp_eq_u32 s95, 7\n"
  "s_cselect_b32 s95, 0, s95\n"
  "s_lshl_b32 s96, s95, 11\n"
  "s_add_u32 s96, s96, s94\n"
  "s_add_u32 s88, %[wl], s96\n"
  "s_addc_u32 s89, %[wh], 0\n"
  "s_add_u32 s88, s88, 14336\n"
  "s_addc_u32 s89, s89, 0\n"
  "global_load_dwordx4 a[80:83], %[l16], s[88:89] offset:0\n"
  "global_load_dwordx4 a[84:87], %[l16], s[88:89] offset:1024\n"
  "s_add_u32 s88, s88, 14336\n"
  "s_addc_u32 s89, s89, 0\n"
  "global_load_dwordx4 a[136:139], %[l16], s[88:89] offset:0\n"
  "global_load_dwordx4 a[140:143], %[l16], s[88:89] offset:1024\n"
  "s_add_u32 s88, s88, 14336\n"
  "s_addc_u32 s89, s89, 0\n"
  "global_load_dwordx4 a[192:195], %[l16], s[88:89] offset:0\n"
  "global_load_dwordx4 a[196:199], %[l16], s[88:89] offset:1024\n"
  "s_add_u32 s88, s88, 14336\n"
  "s_addc_u32 s89, s89, 0\n"
  "global_load_dwordx4 a[248:251], %[l16], s[88:89] offset:0\n"
  "global_load_dwordx4 a[252:255], %[l16], s[88:89] offset:1024\n"
  "s_add_u32 s88, s88, 14336\n"
  "s_addc_u32 s89, s89, 0\n"
  "global_load_dwordx4 v[192:195], %[l16], s[88:89] offset:0\n"
  "global_load_dwordx4 v[196:199], %[l16], s[88:89] offset:1024\n"
  "s_add_u32 s88, s88, 14336\n"
  "s_addc_u32 s89, s89, 0\n"
  "global_load_dwordx4 v[248:251], %[l16], s[88:89] offset:0\n"
  "global_load_dwordx4 v[252:255], %[l16], s[88:89] offset:1024\n"
  "s_add_u32 s95, s95, 1\n"
  "s_cmp_eq_u32 s95, 7\n"
  "s_cselect_b32 s95, 0, s95\n"
  "s_lshl_b32 s95, %[c], 2\n"
  "s_add_u32 s95, s95, %[w]\n"
  "s_mul_i32 s94, s95, 14336\n"
  "s_add_u32 s94, s94, s93\n"
  "s_add_u32 s94, s94, 0x900000\n"
  "s_lshl_b32 s96, %[w], 12\n"
  "s_add_u32 s96, s96, s94\n"
  "s_add_u32 s88, %[wl], s96\n"
  "s_addc_u32 s89, %[wh], 0\n"
  "global_load_dwordx4 v[128:131], %[l16], s[88:89] offset:0\n"
  "global_load_dwordx4 v[132:135], %[l16], s[88:89] offset:1024\n"
  "s_cmp_eq_u32 %[w], 3\n"
  "s_movk_i32 s95, 0x3000\n"
  "s_cselect_b32 s96, s95, -2048\n"
  "s_sub_u32 s88, s88, s96\n"
  "s_subb_u32 s89, s89, 0\n"
  "s_cmp_eq_u32 %[w], 3\n"
  "s_cselect_b32 s96, 0, 1\n"
  "s_add_u32 s89, s89, s96\n"
  "global_load_dwordx4 v[136:139], %[l16], s[88:89] offset:0\n"
  "global_load_dwordx4 v[140:143], %[l16], s[88:89] offset:1024\n"
      : "={a[0:31]}"(ga[0]), "={a[32:63]}"(ga[1]), "={a[64:95]}"(ga[2]), "={a[96:127]}"(ga[3]), "={a[128:159]}"(ga[4]), "={a[160:191]}"(ga[5]),
        "={a[192:223]}"(ga[6]), "={a[224:255]}"(ga[7]), "={v[128:159]}"(gv[0]), "={v[160:191]}"(gv[1]), "={v[192:223]}"(gv[2]), "={v[224:255]}"(gv[3]),
        [ma] "=&v"(a), [mb] "=&v"(b2), [st0] "=&v"(lam), [st1] "=&v"(la2), [st2] "=&v"(rx), [st3] "=&v"(rx2), [dm] "=&{v127}"(dm)
      : [wl] "s"((unsigned)(uintptr_t)ws), [wh] "s"((unsigned)((uintptr_t)ws >> 32)), [h] "s"(h), [c] "s"(c), [w] "s"(w), [l16] "v"(lane * 16), [l4] "v"(lane * 4), [p0] "v"(tid * 128), [p1] "v"(min(tid, 191) * 128 + 32768),
        [mp] "v"(mem0 + (tid & 7) * DM + (tid < 224 ? tid >> 3 : 0) * 16)
      : "s88", "s89", "s93", "s94", "s95", "s96", "scc", "memory");
  asm volatile("s_waitcnt vmcnt(63)" : "+v"(a), "+v"(b2), "+v"(lam), "+v"(la2), "+v"(rx), "+v"(rx2) : : "memory");
  unsigned mx0 = 0;
  if (tid < 224) {
    const int T = tid >> 3, row = tid & 7;
    lds_st<u32x4>(L_MI + T * 256 + row * 32, u32x4{a[0], a[2], b2[0], b2[2]});
    lds_st<u32x4>(L_MI + T * 256 + row * 32 + 16, u32x4{a[1], a[3], b2[1], b2[3]});
#pragma unroll
    for (int i = 0; i < 4; ++i) {
      mx0 = max(mx0, max((a[i] << 16) & 0x7fff0000u, a[i] & 0x7fff0000u));
      mx0 = max(mx0, max((b2[i] << 16) & 0x7fff0000u, b2[i] & 0x7fff0000u));
    }
  }
  lds_st<unsigned>(L_MX + tid * 4, mx0);
  lds_st<u32x4>(L_TB + tid * 32, u32x4{0, 0, 0, 0});
  lds_st<u32x4>(L_TB + tid * 32 + 16, u32x4{0, 0, 0, 0});
  int ex, em;
  {
    rx = fmaxf(rx, rx2);
#pragma unroll
    for (int o = 1; o < 64; o <<= 1) { lam = fmaxf(lam, __shfl_xor(lam, o)); la2 = fmaxf(la2, __shfl_xor(la2, o)); rx = fmaxf(rx, __shfl_xor(rx, o)); }
    lam += la2;
    const float bx = sqrtf(lam) * sqrtf(rx) * 1.03f, bm = sqrtf(lam) * 21.9f;
    ex = (int)(__builtin_bit_cast(unsigned, bx) >> 23) - 126;
    em = (int)(__builtin_bit_cast(unsigned, bm) >> 23) - 252;
  }
  const int b0 = 3 * w, bsh = 12 + (w >> 1);
  const unsigned a_mk = L_MI + (g >> 1) * 256 + (n16 & 7) * 32 + (g & 1) * 8;
  unsigned r0 = (n16 & 2) ? 0xEEEE : 0x4444;
  unsigned r1 = L_MI + w * 2048 + g * 256 + ((n16 >> 2) + 4 * (n16 & 1)) * 32 + ((n16 >> 1) & 1) * 16;
  unsigned r2 = a_mk + b0 * 512, r3 = a_mk + (b0 + 2) * 512, r4 = a_mk + bsh * 512;
  unsigned r5 = L_TB + w * 2048 + g * 128 + ((n16 * 8) ^ ((g >> 1) << 5));
  unsigned r6 = L_TB + w * 2048 + (g >> 1) * 512 + (n16 & 3) * 128 + ((64 * (g & 1) + 8 * (n16 >> 2)) ^ (((n16 >> 1) & 1) << 5));
  unsigned r7 = r6 ^ 32;
  unsigned r8 = L_BS + w * 16384 + lane * 16;
  unsigned r9 = L_SPX + w * 1024 + ((n16 >> 2) * 16 + (g ^ (2 * ((n16 >> 2) & 1)))) * 16 + (n16 & 3) * 4;
  unsigned r10 = L_SPX + (g * 16 + (n16 ^ (2 * (g & 1)))) * 16;
  unsigned r11 = (n16 >= 8 ? L_DUM : L_SPM) + ((n16 & 3) * 16 + g + 8 * ((n16 >> 2) & 1)) * 16 + w * 4;
  unsigned r12 = lane * 16;
  unsigned r13 = L_XS + lane * 16 + b0 * 1024, r14 = L_XS + lane * 16 + bsh * 1024;
  unsigned r15 = c * 2048 + (g & 1) * 1024 + (w * 16 + 4 * (n16 & 3) + 2 * (g >> 1) + ((n16 >> 2) & 1)) * 16 + (n16 >> 3) * 8;
  unsigned r16 = L_MI + (4 * c + w) * 256 + (lane >> 3) * 32 + (((lane & 7) >> 1) + 4 * (lane & 1)) * 4;
  unsigned r17 = (4 * c + w) * 256 + lane * 4;
  asm volatile(
  "s_mov_b64 s[36:37], %[ws]\n"
  "s_mov_b32 s90, %[h]\n"
  "s_mov_b32 s91, %[c]\n"
  "s_mov_b32 s92, %[w]\n"
  "v_readfirstlane_b32 s62, v18\n"
  "v_readfirstlane_b32 s61, v19\n"
  "s_lshl_b32 s95, s91, 2\n"
  "s_add_u32 s95, s95, s92\n"
  "s_mul_i32 s94, s90, 28\n"
  "s_add_u32 s94, s94, s95\n"
  "s_lshl_b32 s94, s94, 11\n"
  "s_add_u32 s94, s94, 0x6e00000\n"
  "s_add_u32 s38, s36, s94\n"
  "s_addc_u32 s39, s37, 0\n"
  "s_add_u32 s46, s36, 0x1200000\n"
  "s_addc_u32 s47, s37, 0\n"
  "s_add_u32 s50, s36, 57344\n"
  "s_addc_u32 s51, s37, 0\n"
  "s_lshl_b32 s94, s95, 2\n"
  "s_add_u32 s94, s94, 17956864\n"
  "s_and_b32 s96, s95, 15\n"
  "s_cmp_eq_u32 s96, s90\n"
  "s_cselect_b64 s[48:49], -1, 0\n"
  "s_cselect_b64 s[76:77], 1, 0\n"
  "s_add_u32 s54, s36, s94\n"
  "s_addc_u32 s55, s37, 0\n"
  "s_add_u32 s36, s36, 0x4000000\n"
  "s_addc_u32 s37, s37, 0\n"
  "s_mov_b32 s40, s36\n"
  "s_and_b32 s41, s37, 0xffff\n"
  "s_mov_b32 s42, 917504\n"
  "s_mov_b32 s43, 0x20000\n"
  "s_add_u32 s36, s36, 14336\n"
  "s_addc_u32 s37, s37, 0\n"
  "s_lshl_b32 s79, s92, 12\n"
  "s_add_u32 s44, s79, 14336\n"
  "s_cmp_ge_u32 s92, 3\n"
  "s_cselect_b32 s74, -7168, 0\n"
  "s_cmp_ge_u32 s92, 2\n"
  "s_cselect_b32 s75, -7168, 0\n"
  "s_cmp_ge_u32 s92, 1\n"
  "s_cselect_b32 s78, -7168, 0\n"
  "s_lshl_b32 s70, s74, 1\n"
  "s_add_u32 s70, s70, s44\n"
  "s_add_u32 s70, s70, 2048\n"
  "s_mul_i32 s80, s92, 7168\n"
  "s_add_u32 s80, s80, 3072\n"
  "s_mov_b32 s52, s80\n"
  "s_sub_u32 s50, s50, 28672\n"
  "s_subb_u32 s51, s51, 0\n"
  "s_add_u32 s50, s50, s80\n"
  "s_addc_u32 s51, s51, 0\n"
  "s_add_u32 s83, s80, 0x8000\n"
  "s_mov_b32 m0, s83\n"
  "s_mov_b32 s56, 0\n"
  "s_mov_b32 s57, 0x3f800000\n"
  "s_mov_b32 s58, 0x3fb8aa3b\n"
  "s_mov_b32 s59, 0x3fb8aa3b\n"
  "s_max_i32 s62, s62, -28\n"
  "s_mov_b32 s63, s61\n"
  "s_sub_i32 s61, s61, 22\n"
  "s_mov_b32 s64, 0x8000\n"
  "s_mov_b32 s66, 0\n"
  "s_mov_b32 s72, 0xff00ff00\n"
  "s_mov_b32 s73, 0xff00ff00\n"
  "s_mov_b32 s81, 0\n"
  "s_and_b32 s82, s92, 1\n"
  "s_lshl_b32 s82, s82, 2\n"
  "s_lshl_b32 s87, s92, 2\n"
  "s_mov_b32 s84, 0xfc000000\n"
  "s_mov_b32 s85, 0x4b000000\n"
  "v_mov_b32 v51, 0\n"
  "v_mov_b32 v55, 0\n"
  "s_waitcnt vmcnt(0) lgkmcnt(0)\n"
  "s_barrier\n"
  "s_add_u32 s83, s81, s79\n"
  "v_add_u32 v20, s83, v12\n"
  "v_lshl_add_u32 v21, s74, 1, v20\n"
  "v_lshl_add_u32 v22, s75, 1, v20\n"
  "v_lshl_add_u32 v23, s78, 1, v20\n"
  "ds_read_b128 v[36:39], v20 offset:0\n"
  "ds_read_b128 v[40:43], v20 offset:14336\n"
  "ds_read_b128 v[44:47], v20 offset:1024\n"
  "ds_read_b128 v[92:95], v20 offset:15360\n"
  "ds_read_b128 v[96:99], v21 offset:2048\n"
  "ds_read_b128 v[100:103], v21 offset:16384\n"
  "ds_read_b128 v[104:107], v21 offset:3072\n"
  "ds_read_b128 v[108:111], v21 offset:17408\n"
  "ds_read_b128 v[64:67], v8 offset:0\n"
  "ds_read_b128 v[112:115], v21 offset:4096\n"
  "ds_read_b128 v[116:119], v21 offset:18432\n"
  "ds_read_b128 v[68:71], v8 offset:1024\n"
  "ds_read_b128 v[120:123], v21 offset:5120\n"
  "ds_read_b128 v[124:127], v21 offset:19456\n"
  "s_waitcnt lgkmcnt(12)\n"
  "v_mfma_f32_16x16x32_bf16 v[56:59], v[36:39], v[128:131], 0\n"
  "v_mfma_f32_16x16x32_bf16 v[60:63], v[40:43], v[128:131], 0\n"
  "ds_read_b128 v[72:75], v8 offset:2048\n"
  "ds_read_b128 v[36:39], v22 offset:6144\n"
  "ds_read_b128 v[40:43], v22 offset:20480\n"
  "s_waitcnt lgkmcnt(13)\n"
  "v_mfma_f32_16x16x32_bf16 v[56:59], v[44:47], v[132:135], v[56:59]\n"
  "v_mfma_f32_16x16x32_bf16 v[60:63], v[92:95], v[132:135], v[60:63]\n"
  "ds_read_b128 v[76:79], v8 offset:3072\n"
  "ds_read_b128 v[44:47], v22 offset:7168\n"
  "ds_read_b128 v[92:95], v22 offset:21504\n"
  "s_waitcnt lgkmcnt(14)\n"
  "v_mfma_f32_16x16x32_bf16 v[56:59], v[96:99], v[136:139], v[56:59]\n"
  "v_mfma_f32_16x16x32_bf16 v[60:63], v[100:103], v[136:139], v[60:63]\n"
  "ds_read_b128 v[80:83], v8 offset:4096\n"
  "ds_read_b128 v[96:99], v22 offset:8192\n"
  "ds_read_b128 v[100:103], v22 offset:22528\n"
  "s_waitcnt lgkmcnt(15)\n"
  "v_mfma_f32_16x16x32_bf16 v[56:59], v[104:107], v[140:143], v[56:59]\n"
  "v_mfma_f32_16x16x32_bf16 v[60:63], v[108:111], v[140:143], v[60:63]\n"
  "ds_read_b128 v[84:87], v8 offset:5120\n"
  "ds_read_b128 v[104:107], v22 offset:9216\n"
  "ds_read_b128 v[108:111], v22 offset:23552\n"
  "s_waitcnt lgkmcnt(15)\n"
  "v_mfma_f32_16x16x32_bf16 v[56:59], v[112:115], v[64:67], v[56:59]\n"
  "v_mfma_f32_16x16x32_bf16 v[60:63], v[116:119], v[64:67], v[60:63]\n"
  "ds_read_b128 v[88:91], v8 offset:6144\n"
  "ds_read_b128 v[112:115], v23 offset:10240\n"
  "ds_read_b128 v[116:119], v23 offset:24576\n"
  "s_waitcnt lgkmcnt(15)\n"
  "v_mfma_f32_16x16x32_bf16 v[56:59], v[120:123], v[68:71], v[56:59]\n"
  "v_mfma_f32_16x16x32_bf16 v[60:63], v[124:127], v[68:71], v[60:63]\n"
  "ds_read_b128 v[64:67], v8 offset:7168\n"
  "ds_read_b128 v[120:123], v23 offset:11264\n"
  "ds_read_b128 v[124:127], v23 offset:25600\n"
  "s_waitcnt lgkmcnt(15)\n"
  "v_mfma_f32_16x16x32_bf16 v[56:59], v[36:39], v[72:75], v[56:59]\n"
  "v_mfma_f32_16x16x32_bf16 v[60:63], v[40:43], v[72:75], v[60:63]\n"
  "ds_read_b128 v[68:71], v8 offset:8192\n"
  "ds_read_b128 v[36:39], v23 offset:12288\n"
  "ds_read_b128 v[40:43], v23 offset:26624\n"
  "s_waitcnt lgkmcnt(15)\n"
  "v_mfma_f32_16x16x32_bf16 v[56:59], v[44:47], v[76:79], v[56:59]\n"
  "v_mfma_f32_16x16x32_bf16 v[60:63], v[92:95], v[76:79], v[60:63]\n"
  "ds_read_b128 v[72:75], v8 offset:9216\n"
  "ds_read_b128 v[44:47], v23 offset:13312\n"
  "ds_read_b128 v[92:95], v23 offset:27648\n"
  "s_waitcnt lgkmcnt(15)\n"
  "v_mfma_f32_16x16x32_bf16 v[56:59], v[96:99], v[80:83], v[56:59]\n"
  "v_mfma_f32_16x16x32_bf16 v[60:63], v[100:103], v[80:83], v[60:63]\n"
  "s_waitcnt lgkmcnt(12)\n"
  "v_mfma_f32_16x16x32_bf16 v[56:59], v[104:107], v[84:87], v[56:59]\n"
  "v_mfma_f32_16x16x32_bf16 v[60:63], v[108:111], v[84:87], v[60:63]\n"
  "s_waitcnt lgkmcnt(9)\n"
  "v_mfma_f32_16x16x32_bf16 v[56:59], v[112:115], v[88:91], v[56:59]\n"
  "v_mfma_f32_16x16x32_bf16 v[60:63], v[116:119], v[88:91], v[60:63]\n"
  "s_waitcnt lgkmcnt(6)\n"
  "v_mfma_f32_16x16x32_bf16 v[56:59], v[120:123], v[64:67], v[56:59]\n"
  "v_mfma_f32_16x16x32_bf16 v[60:63], v[124:127], v[64:67], v[60:63]\n"
  "s_waitcnt lgkmcnt(3)\n"
  "v_mfma_f32_16x16x32_bf16 v[56:59], v[36:39], v[68:71], v[56:59]\n"
  "v_mfma_f32_16x16x32_bf16 v[60:63], v[40:43], v[68:71], v[60:63]\n"
  "s_waitcnt lgkmcnt(0)\n"
  "v_mfma_f32_16x16x32_bf16 v[56:59], v[44:47], v[72:75], v[56:59]\n"
  "v_mfma_f32_16x16x32_bf16 v[60:63], v[92:95], v[72:75], v[60:63]\n"
  "ds_read_b128 v[92:95], v8 offset:10240\n"
  "ds_read_b128 v[96:99], v8 offset:11264\n"
  "ds_read_b128 v[100:103], v8 offset:12288\n"
  "ds_read_b128 v[104:107], v8 offset:13312\n"
  "ds_read_b128 v[108:111], v8 offset:14336\n"
  "ds_read_b128 v[112:115], v8 offset:15360\n"
  "v_mov_b64 v[64:65], 0\n"
  "v_mov_b64 v[66:67], 0\n"
  "v_mov_b64 v[68:69], 0\n"
  "v_mov_b64 v[70:71], 0\n"
  "v_mov_b64 v[72:73], 0\n"
  "v_mov_b64 v[74:75], 0\n"
  "v_mov_b64 v[76:77], 0\n"
  "v_mov_b64 v[78:79], 0\n"
  "v_mov_b64 v[80:81], 0\n"
  "v_mov_b64 v[82:83], 0\n"
  "v_mov_b64 v[84:85], 0\n"
  "v_mov_b64 v[86:87], 0\n"
  "v_mov_b64 v[88:89], 0\n"
  "v_mov_b64 v[90:91], 0\n"
  "v_mov_b64 v[120:121], 0\n"
  "v_mov_b64 v[122:123], 0\n"
  "v_mov_b64 v[124:125], 0\n"
  "v_mov_b64 v[126:127], 0\n"
  "v_cvt_pk_bf16_f32 v48, v56, v57\n"
  "v_cvt_pk_bf16_f32 v49, v58, v59\n"
  "v_cvt_pk_bf16_f32 v52, v60, v61\n"
  "v_cvt_pk_bf16_f32 v53, v62, v63\n"
  "v_mov_b64 v[56:57], 0\n"
  "v_mov_b64 v[58:59], 0\n"
  "v_mov_b64 v[60:61], 0\n"
  "v_mov_b64 v[62:63], 0\n"
  "s_mov_b32 s81, 0x8000\n"
  "v_add_u32 v18, s74, v1\n"
  "v_add_u32 v19, s75, v1\n"
  "v_add_u32 v119, s78, v1\n"
  "v_add_u32 v118, s87, v12\n"
  "ds_read_b128 v[20:23], v1\n"
  "ds_read_b128 v[24:27], v18 offset:1024\n"
  "ds_read_b32 v117, v118 offset:63488\n"
  "v_mov_b64 v[44:45], 0\n"
  "v_mov_b64 v[46:47], 0\n"
  "s_waitcnt lgkmcnt(0)\n"
  "s_nop 1\n"
  "CH_LOOP%=:\n"
  "v_smfmac_f32_16x16x64_bf16 v[56:59], v[20:23], a[0:7], v0\n"
  "v_max3_f32 v117, |v44|, |v45|, v117\n"
  "v_max3_f32 v117, |v46|, |v47|, v117\n"
  "ds_write_b32 v118, v117 offset:63488\n"
  "v_smfmac_f32_16x16x64_bf16 v[60:63], v[20:23], a[32:39], v0\n"
  "global_load_lds_dwordx4 v12, s[50:51] offset:-3072\n"
  "v_smfmac_f32_16x16x64_bf16 v[64:67], v[20:23], a[88:95], v0\n"
  "v_smfmac_f32_16x16x64_bf16 v[68:71], v[20:23], a[144:151], v0\n"
  "global_load_lds_dwordx4 v12, s[50:51] offset:-2048\n"
  "v_smfmac_f32_16x16x64_bf16 v[72:75], v[20:23], a[200:207], v0\n"
  "v_smfmac_f32_16x16x64_bf16 v[76:79], v[20:23], v[144:151], v0\n"
  "global_load_lds_dwordx4 v12, s[50:51] offset:-1024\n"
  "v_smfmac_f32_16x16x64_bf16 v[120:123], v[20:23], v[200:207], v0\n"
  "s_waitcnt lgkmcnt(0)\n"
  "s_barrier\n"
  "ds_read_b128 v[28:31], v18 offset:2048\n"
  "ds_read_b128 v[32:35], v19 offset:3072\n"
  "ds_read_b128 v[36:39], v19 offset:4096\n"
  "ds_read_b128 v[40:43], v119 offset:5120\n"
  "ds_read_b128 v[44:47], v119 offset:6144\n"
  "ds_read_b128 v[116:119], v12 offset:63488\n"
  "ds_read_b32 v18, v16\n"
  "s_cmp_eq_u32 s56, 63\n"
  "s_cbranch_scc1 CH_TAIL%=\n"
  "v_smfmac_f32_16x16x64_bf16 v[56:59], v[24:27], a[8:15], v0\n"
  "global_load_lds_dwordx4 v12, s[50:51] offset:0\n"
  "v_smfmac_f32_16x16x64_bf16 v[60:63], v[24:27], a[40:47], v0\n"
  "v_smfmac_f32_16x16x64_bf16 v[64:67], v[24:27], a[96:103], v0\n"
  "global_load_lds_dwordx4 v12, s[50:51] offset:1024\n"
  "v_smfmac_f32_16x16x64_bf16 v[68:71], v[24:27], a[152:159], v0\n"
  "v_smfmac_f32_16x16x64_bf16 v[72:75], v[24:27], a[208:215], v0\n"
  "global_load_lds_dwordx4 v12, s[50:51] offset:2048\n"
  "v_smfmac_f32_16x16x64_bf16 v[76:79], v[24:27], v[152:159], v0\n"
  "v_smfmac_f32_16x16x64_bf16 v[120:123], v[24:27], v[208:215], v0\n"
  "global_load_lds_dwordx4 v12, s[50:51] offset:3072\n"
  "s_waitcnt lgkmcnt(6)\n"
  "v_smfmac_f32_16x16x64_bf16 v[56:59], v[28:31], a[16:23], v0\n"
  "v_smfmac_f32_16x16x64_bf16 v[60:63], v[28:31], a[48:55], v0\n"
  "s_waitcnt lgkmcnt(4)\n"
  "v_smfmac_f32_16x16x64_bf16 v[56:59], v[32:35], a[24:31], v0\n"
  "v_smfmac_f32_16x16x64_bf16 v[60:63], v[32:35], a[56:63], v0\n"
  "s_waitcnt lgkmcnt(0)\n"
  "v_max3_u32 v116, v116, v117, v118\n"
  "v_max_u32 v116, v116, v119\n"
  "v_smfmac_f32_16x16x64_bf16 v[56:59], v[36:39], v[92:99], v0\n"
  "v_lshlrev_b32 v19, 16, v18\n"
  "v_and_b32 v18, 0xffff0000, v18\n"
  "v_smfmac_f32_16x16x64_bf16 v[60:63], v[36:39], a[64:71], v0\n"
  "v_max_u32_dpp v116, v116, v116 row_ror:1 row_mask:0xf bank_mask:0xf\n"
  "v_mul_f32 v19, s57, v19\n"
  "v_mul_f32 v18, s57, v18\n"
  "v_smfmac_f32_16x16x64_bf16 v[56:59], v[40:43], v[100:107], v0\n"
  "v_max_u32_dpp v116, v116, v116 row_ror:2 row_mask:0xf bank_mask:0xf\n"
  "v_cvt_pk_bf16_f32 v18, v19, v18\n"
  "v_smfmac_f32_16x16x64_bf16 v[60:63], v[40:43], a[72:79], v0\n"
  "v_max_u32_dpp v116, v116, v116 row_ror:4 row_mask:0xf bank_mask:0xf\n"
  "s_mov_b64 exec, s[48:49]\n"
  "global_store_dword v17, v18, s[46:47] sc1\n"
  "v_smfmac_f32_16x16x64_bf16 v[56:59], v[44:47], v[108:115], v0\n"
  "v_max_u32_dpp v116, v116, v116 row_ror:8 row_mask:0xf bank_mask:0xf\n"
  "s_mov_b64 exec, -1\n"
  "v_smfmac_f32_16x16x64_bf16 v[60:63], v[44:47], a[80:87], v0\n"
  "v_max_u32_dpp v116, v116, v116 row_bcast:15 row_mask:0xa bank_mask:0xf\n"
  "v_smfmac_f32_16x16x64_bf16 v[64:67], v[28:31], a[104:111], v0\n"
  "ds_read_b128 v[92:95], v13\n"
  "ds_read_b128 v[96:99], v13 offset:1024\n"
  "v_max_u32_dpp v116, v116, v116 row_bcast:31 row_mask:0xc bank_mask:0xf\n"
  "v_smfmac_f32_16x16x64_bf16 v[68:71], v[28:31], a[160:167], v0\n"
  "ds_read_b128 v[100:103], v13 offset:14336\n"
  "ds_read_b128 v[104:107], v13 offset:15360\n"
  "v_smfmac_f32_16x16x64_bf16 v[64:67], v[32:35], a[112:119], v0\n"
  "ds_read2_b32 v[108:109], v2 offset0:0 offset1:4\n"
  "ds_read2_b32 v[110:111], v2 offset0:1 offset1:5\n"
  "v_readlane_b32 s83, v116, 63\n"
  "v_smfmac_f32_16x16x64_bf16 v[68:71], v[32:35], a[168:175], v0\n"
  "v_add_f32 v56, v56, v58\n"
  "v_add_f32 v57, v57, v59\n"
  "ds_read2_b32 v[112:113], v2 offset0:128 offset1:132\n"
  "v_smfmac_f32_16x16x64_bf16 v[64:67], v[36:39], a[120:127], v0\n"
  "v_add_f32 v60, v60, v62\n"
  "v_add_f32 v61, v61, v63\n"
  "ds_read2_b32 v[114:115], v2 offset0:129 offset1:133\n"
  "v_smfmac_f32_16x16x64_bf16 v[68:71], v[36:39], a[176:183], v0\n"
  "v_cvt_pk_bf16_f32 v58, v56, v57\n"
  "v_cvt_pk_bf16_f32 v59, v60, v61\n"
  "v_smfmac_f32_16x16x64_bf16 v[64:67], v[40:43], a[128:135], v0\n"
  "ds_write_b64 v5, v[58:59] offset:0\n"
  "s_lshr_b32 s83, s83, 23\n"
  "s_add_i32 s83, s83, s63\n"
  "v_smfmac_f32_16x16x64_bf16 v[68:71], v[40:43], a[184:191], v0\n"
  "s_max_i32 s83, s83, s62\n"
  "s_min_i32 s86, s83, 72\n"
  "v_smfmac_f32_16x16x64_bf16 v[64:67], v[44:47], a[136:143], v0\n"
  "s_sub_i32 s83, 149, s86\n"
  "s_lshl_b32 s60, s83, 23\n"
  "v_smfmac_f32_16x16x64_bf16 v[68:71], v[44:47], a[192:199], v0\n"
  "ds_read_b128 v[56:59], v8 offset:0\n"
  "ds_read_b128 v[60:63], v8 offset:1024\n"
  "v_smfmac_f32_16x16x64_bf16 v[72:75], v[28:31], a[216:223], v0\n"
  "v_smfmac_f32_16x16x64_bf16 v[76:79], v[28:31], v[160:167], v0\n"
  "s_nop 1\n"
  "v_smfmac_f32_16x16x64_bf16 v[72:75], v[32:35], a[224:231], v0\n"
  "v_add_f32 v64, v64, v66\n"
  "v_add_f32 v65, v65, v67\n"
  "v_smfmac_f32_16x16x64_bf16 v[76:79], v[32:35], v[168:175], v0\n"
  "v_add_f32 v68, v68, v70\n"
  "v_add_f32 v69, v69, v71\n"
  "v_smfmac_f32_16x16x64_bf16 v[72:75], v[36:39], a[232:239], v0\n"
  "v_cvt_pk_bf16_f32 v66, v64, v65\n"
  "v_cvt_pk_bf16_f32 v67, v68, v69\n"
  "v_smfmac_f32_16x16x64_bf16 v[76:79], v[36:39], v[176:183], v0\n"
  "ds_write_b64 v5, v[66:67] offset:512\n"
  "ds_read_b64_tr_b16 v[116:117], v6 offset:0\n"
  "v_smfmac_f32_16x16x64_bf16 v[72:75], v[40:43], a[240:247], v0\n"
  "ds_read_b64_tr_b16 v[118:119], v7 offset:0\n"
  "v_smfmac_f32_16x16x64_bf16 v[76:79], v[40:43], v[184:191], v0\n"
  "ds_read_b128 v[64:67], v8 offset:2048\n"
  "ds_read_b128 v[68:71], v8 offset:3072\n"
  "v_smfmac_f32_16x16x64_bf16 v[72:75], v[44:47], a[248:255], v0\n"
  "v_smfmac_f32_16x16x64_bf16 v[76:79], v[44:47], v[192:199], v0\n"
  "s_waitcnt lgkmcnt(2)\n"
  "v_smfmac_f32_16x16x64_bf16 v[80:83], v[116:119], v[92:99], v0\n"
  "v_smfmac_f32_16x16x64_bf16 v[84:87], v[116:119], v[100:107], v0\n"
  "ds_read_b128 v[92:95], v13 offset:2048\n"
  "ds_read_b128 v[96:99], v14\n"
  "v_smfmac_f32_16x16x64_bf16 v[88:91], v[116:119], v[108:115], v0\n"
  "ds_read_b128 v[100:103], v13 offset:16384\n"
  "ds_read_b128 v[104:107], v14 offset:14336\n"
  "v_smfmac_f32_16x16x64_bf16 v[120:123], v[28:31], v[216:223], v0\n"
  "ds_read2_b32 v[108:109], v3 offset0:0 offset1:4\n"
  "ds_read2_b32 v[110:111], v3 offset0:1 offset1:5\n"
  "v_add_f32 v72, v72, v74\n"
  "v_add_f32 v73, v73, v75\n"
  "v_smfmac_f32_16x16x64_bf16 v[120:123], v[32:35], v[224:231], v0\n"
  "ds_read2_b32 v[112:113], v4 offset0:0 offset1:4\n"
  "ds_read2_b32 v[114:115], v4 offset0:1 offset1:5\n"
  "v_add_f32 v76, v76, v78\n"
  "v_add_f32 v77, v77, v79\n"
  "v_smfmac_f32_16x16x64_bf16 v[120:123], v[36:39], v[232:239], v0\n"
  "v_cvt_pk_bf16_f32 v74, v72, v73\n"
  "v_cvt_pk_bf16_f32 v75, v76, v77\n"
  "v_add_u32 v116, s82, v5\n"
  "v_smfmac_f32_16x16x64_bf16 v[120:123], v[40:43], v[240:247], v0\n"
  "ds_write_b64 v5, v[74:75] offset:1024\n"
  "ds_read_b128 v[72:75], v8 offset:4096\n"
  "v_smfmac_f32_16x16x64_bf16 v[120:123], v[44:47], v[248:255], v0\n"
  "ds_read_b128 v[76:79], v8 offset:5120\n"
  "v_smfmac_f32_16x16x64_bf16 v[124:127], v[28:31], v[56:63], v0\n"
  "ds_read_b128 v[56:59], v8 offset:6144\n"
  "ds_read_b128 v[60:63], v8 offset:7168\n"
  "v_smfmac_f32_16x16x64_bf16 v[124:127], v[32:35], v[64:71], v0\n"
  "ds_read_b128 v[64:67], v8 offset:8192\n"
  "ds_read_b128 v[68:71], v8 offset:9216\n"
  "s_nop 0\n"
  "v_add_f32 v120, v120, v122\n"
  "v_add_f32 v121, v121, v123\n"
  "v_cvt_pk_bf16_f32 v122, v120, v121\n"
  "ds_write_b32 v116, v122 offset:1536\n"
  "ds_read_b64_tr_b16 v[116:117], v6 offset:1024\n"
  "ds_read_b64_tr_b16 v[118:119], v7 offset:1024\n"
  "v_smfmac_f32_16x16x64_bf16 v[124:127], v[20:23], v[128:135], v0\n"
  "v_smfmac_f32_16x16x64_bf16 v[124:127], v[24:27], v[136:143], v0\n"
  "s_waitcnt lgkmcnt(3)\n"
  "v_smfmac_f32_16x16x64_bf16 v[124:127], v[36:39], v[72:79], v0\n"
  "v_smfmac_f32_16x16x64_bf16 v[124:127], v[40:43], v[56:63], v0\n"
  "s_waitcnt lgkmcnt(0)\n"
  "v_smfmac_f32_16x16x64_bf16 v[80:83], v[116:119], v[92:99], v0\n"
  "v_smfmac_f32_16x16x64_bf16 v[84:87], v[116:119], v[100:107], v0\n"
  "v_smfmac_f32_16x16x64_bf16 v[88:91], v[116:119], v[108:115], v0\n"
  "s_nop 5\n"
  "v_add_f32 v80, v80, v82\n"
  "v_add_f32 v81, v81, v83\n"
  "v_add_f32 v84, v84, v86\n"
  "v_add_f32 v85, v85, v87\n"
  "ds_write2_b32 v9, v80, v81 offset0:0 offset1:16\n"
  "v_add_f32 v88, v88, v90\n"
  "v_add_f32 v89, v89, v91\n"
  "ds_write2_b32 v9, v84, v85 offset0:32 offset1:48\n"
  "ds_write2_b32 v11, v88, v89 offset0:0 offset1:16\n"
  "v_smfmac_f32_16x16x64_bf16 v[124:127], v[44:47], v[64:71], v0\n"
  "s_waitcnt vmcnt(0)\n"
  "s_mov_b64 exec, s[76:77]\n"
  "global_store_dword v12, v0, s[54:55] sc1\n"
  "s_mov_b64 exec, -1\n"
  "s_add_u32 s54, s54, 128\n"
  "s_addc_u32 s55, s55, 0\n"
  "s_waitcnt lgkmcnt(0)\n"
  "s_barrier\n"
  "ds_read_b128 v[92:95], v10 offset:0\n"
  "ds_read_b128 v[96:99], v10 offset:1024\n"
  "ds_read_b128 v[100:103], v10 offset:2048\n"
  "ds_read_b128 v[104:107], v10 offset:3072\n"
  "ds_read_b128 v[108:111], v12 offset:61440\n"
  "v_pk_add_f32 v[124:125], v[124:125], v[126:127]\n"
  "v_mov_b32 v115, 0\n"
  "v_mov_b32 v116, 0x4b7fffff\n"
  "v_cvt_pk_bf16_f32 v50, v124, 0\n"
  "v_cvt_pk_bf16_f32 v54, v125, 0\n"
  "s_waitcnt lgkmcnt(3)\n"
  "v_pk_add_f32 v[92:93], v[92:93], v[96:97]\n"
  "v_pk_add_f32 v[94:95], v[94:95], v[98:99]\n"
  "s_waitcnt lgkmcnt(1)\n"
  "v_pk_add_f32 v[100:101], v[100:101], v[104:105]\n"
  "v_pk_add_f32 v[102:103], v[102:103], v[106:107]\n"
  "v_pk_add_f32 v[92:93], v[92:93], v[100:101]\n"
  "v_pk_add_f32 v[94:95], v[94:95], v[102:103]\n"
  "s_waitcnt lgkmcnt(0)\n"
  "v_pk_add_f32 v[108:109], v[108:109], v[110:111]\n"
  "v_max3_f32 v96, v92, v93, v94\n"
  "v_add_f32 v108, v108, v109\n"
  "v_max_f32 v96, v96, v95\n"
  "v_mul_f32 v108, s59, v108\n"
  "v_mul_f32 v96, s58, v96\n"
  "v_max_f32 v96, v96, v108\n"
  "s_nop 1\n"
  "v_max_f32_dpp v96, v96, v96 row_ror:8 row_mask:0xf bank_mask:0xf\n"
  "v_mov_b32 v97, v96\n"
  "s_nop 1\n"
  "v_permlane16_swap_b32 v96, v97\n"
  "v_max_f32 v96, v96, v97\n"
  "v_mov_b32 v97, v96\n"
  "s_nop 1\n"
  "v_permlane32_swap_b32 v96, v97\n"
  "v_max_f32 v96, v96, v97\n"
  "v_fma_f32 v92, v92, s58, -v96\n"
  "v_fma_f32 v93, v93, s58, -v96\n"
  "v_fma_f32 v94, v94, s58, -v96\n"
  "v_fma_f32 v95, v95, s58, -v96\n"
  "v_sub_f32 v108, v108, v96\n"
  "v_exp_f32 v92, v92\n"
  "v_exp_f32 v93, v93\n"
  "v_exp_f32 v94, v94\n"
  "v_exp_f32 v95, v95\n"
  "v_exp_f32 v108, v108\n"
  "v_cvt_pk_bf16_f32 v112, v92, v93\n"
  "v_cvt_pk_bf16_f32 v113, v94, v95\n"
  "v_mul_f32 v98, s57, v108\n"
  "v_cvt_pk_bf16_f32 v114, v98, 0\n"
  "v_pk_add_f32 v[92:93], v[92:93], v[94:95]\n"
  "v_add_f32 v108, v108, v92\n"
  "v_mfma_f32_16x16x32_bf16 v[56:59], v[48:51], v[112:115], 0\n"
  "v_add_f32 v108, v108, v93\n"
  "v_mfma_f32_16x16x32_bf16 v[60:63], v[52:55], v[112:115], 0\n"
  "s_nop 0\n"
  "v_add_f32_dpp v108, v108, v108 row_ror:8 row_mask:0xf bank_mask:0xf\n"
  "v_mov_b32 v97, v108\n"
  "s_nop 1\n"
  "v_permlane16_swap_b32 v108, v97\n"
  "v_add_f32 v108, v108, v97\n"
  "v_mov_b32 v97, v108\n"
  "v_cndmask_b32 v100, v58, v60, s[72:73]\n"
  "v_cndmask_b32 v101, v59, v61, s[72:73]\n"
  "v_permlane32_swap_b32 v108, v97\n"
  "v_add_f32 v108, v108, v97\n"
  "v_rcp_f32 v108, v108\n"
  "v_cndmask_b32 v102, v56, v62, s[72:73]\n"
  "v_cndmask_b32 v103, v57, v63, s[72:73]\n"
  "v_mul_f32 v108, s60, v108\n"
  "v_add_f32_dpp v102, v100, v102 row_ror:8 row_mask:0xf bank_mask:0xf\n"
  "v_add_f32_dpp v103, v101, v103 row_ror:8 row_mask:0xf bank_mask:0xf\n"
  "v_fmaak_f32 v102, v102, v108, 0x4b400000\n"
  "v_fmaak_f32 v103, v103, v108, 0x4b400000\n"
  "v_med3_i32 v102, v102, s85, v116\n"
  "v_med3_i32 v103, v103, s85, v116\n"
  "v_add_u32 v102, 0xbcc00000, v102\n"
  "v_add_u32 v103, 0xbcc00000, v103\n"
  "global_atomic_add_x2 v15, v[102:103], s[36:37]\n"
  "s_add_u32 s50, s50, 28672\n"
  "s_addc_u32 s51, s51, 0\n"
  "v_lshlrev_b32 v47, 3, v12\n"
  "s_mov_b32 m0, 65536\n"
  "s_nop 0\n"
  "global_load_lds_dword v47, s[50:51] offset:-3072\n"
  "s_add_u32 s83, s81, s79\n"
  "v_add_u32 v20, s83, v12\n"
  "v_lshl_add_u32 v21, s74, 1, v20\n"
  "v_lshl_add_u32 v22, s75, 1, v20\n"
  "v_lshl_add_u32 v23, s78, 1, v20\n"
  "ds_read_b128 v[36:39], v20 offset:0\n"
  "ds_read_b128 v[40:43], v20 offset:14336\n"
  "ds_read_b128 v[44:47], v20 offset:1024\n"
  "ds_read_b128 v[92:95], v20 offset:15360\n"
  "ds_read_b128 v[96:99], v21 offset:2048\n"
  "ds_read_b128 v[100:103], v21 offset:16384\n"
  "ds_read_b128 v[104:107], v21 offset:3072\n"
  "ds_read_b128 v[108:111], v21 offset:17408\n"
  "ds_read_b128 v[64:67], v8 offset:0\n"
  "ds_read_b128 v[112:115], v21 offset:4096\n"
  "ds_read_b128 v[116:119], v21 offset:18432\n"
  "ds_read_b128 v[68:71], v8 offset:1024\n"
  "ds_read_b128 v[120:123], v21 offset:5120\n"
  "ds_read_b128 v[124:127], v21 offset:19456\n"
  "s_waitcnt lgkmcnt(12)\n"
  "v_mfma_f32_16x16x32_bf16 v[56:59], v[36:39], v[128:131], 0\n"
  "v_mfma_f32_16x16x32_bf16 v[60:63], v[40:43], v[128:131], 0\n"
  "ds_read_b128 v[72:75], v8 offset:2048\n"
  "ds_read_b128 v[36:39], v22 offset:6144\n"
  "ds_read_b128 v[40:43], v22 offset:20480\n"
  "s_waitcnt lgkmcnt(13)\n"
  "v_mfma_f32_16x16x32_bf16 v[56:59], v[44:47], v[132:135], v[56:59]\n"
  "v_mfma_f32_16x16x32_bf16 v[60:63], v[92:95], v[132:135], v[60:63]\n"
  "ds_read_b128 v[76:79], v8 offset:3072\n"
  "ds_read_b128 v[44:47], v22 offset:7168\n"
  "ds_read_b128 v[92:95], v22 offset:21504\n"
  "s_waitcnt lgkmcnt(14)\n"
  "v_mfma_f32_16x16x32_bf16 v[56:59], v[96:99], v[136:139], v[56:59]\n"
  "v_mfma_f32_16x16x32_bf16 v[60:63], v[100:103], v[136:139], v[60:63]\n"
  "ds_read_b128 v[80:83], v8 offset:4096\n"
  "ds_read_b128 v[96:99], v22 offset:8192\n"
  "ds_read_b128 v[100:103], v22 offset:22528\n"
  "s_waitcnt lgkmcnt(15)\n"
  "v_mfma_f32_16x16x32_bf16 v[56:59], v[104:107], v[140:143], v[56:59]\n"
  "v_mfma_f32_16x16x32_bf16 v[60:63], v[108:111], v[140:143], v[60:63]\n"
  "ds_read_b128 v[84:87], v8 offset:5120\n"
  "ds_read_b128 v[104:107], v22 offset:9216\n"
  "ds_read_b128 v[108:111], v22 offset:23552\n"
  "s_waitcnt lgkmcnt(15)\n"
  "v_mfma_f32_16x16x32_bf16 v[56:59], v[112:115], v[64:67], v[56:59]\n"
  "v_mfma_f32_16x16x32_bf16 v[60:63], v[116:119], v[64:67], v[60:63]\n"
  "ds_read_b128 v[88:91], v8 offset:6144\n"
  "ds_read_b128 v[112:115], v23 offset:10240\n"
  "ds_read_b128 v[116:119], v23 offset:24576\n"
  "s_waitcnt lgkmcnt(15)\n"
  "v_mfma_f32_16x16x32_bf16 v[56:59], v[120:123], v[68:71], v[56:59]\n"
  "v_mfma_f32_16x16x32_bf16 v[60:63], v[124:127], v[68:71], v[60:63]\n"
  "ds_read_b128 v[64:67], v8 offset:7168\n"
  "ds_read_b128 v[120:123], v23 offset:11264\n"
  "ds_read_b128 v[124:127], v23 offset:25600\n"
  "s_waitcnt lgkmcnt(15)\n"
  "v_mfma_f32_16x16x32_bf16 v[56:59], v[36:39], v[72:75], v[56:59]\n"
  "v_mfma_f32_16x16x32_bf16 v[60:63], v[40:43], v[72:75], v[60:63]\n"
  "ds_read_b128 v[68:71], v8 offset:8192\n"
  "ds_read_b128 v[36:39], v23 offset:12288\n"
  "ds_read_b128 v[40:43], v23 offset:26624\n"
  "s_waitcnt lgkmcnt(15)\n"
  "v_mfma_f32_16x16x32_bf16 v[56:59], v[44:47], v[76:79], v[56:59]\n"
  "v_mfma_f32_16x16x32_bf16 v[60:63], v[92:95], v[76:79], v[60:63]\n"
  "ds_read_b128 v[72:75], v8 offset:9216\n"
  "ds_read_b128 v[44:47], v23 offset:13312\n"
  "ds_read_b128 v[92:95], v23 offset:27648\n"
  "s_waitcnt lgkmcnt(15)\n"
  "v_mfma_f32_16x16x32_bf16 v[56:59], v[96:99], v[80:83], v[56:59]\n"
  "v_mfma_f32_16x16x32_bf16 v[60:63], v[100:103], v[80:83], v[60:63]\n"
  "s_waitcnt lgkmcnt(12)\n"
  "v_mfma_f32_16x16x32_bf16 v[56:59], v[104:107], v[84:87], v[56:59]\n"
  "v_mfma_f32_16x16x32_bf16 v[60:63], v[108:111], v[84:87], v[60:63]\n"
  "s_waitcnt lgkmcnt(9)\n"
  "v_mfma_f32_16x16x32_bf16 v[56:59], v[112:115], v[88:91], v[56:59]\n"
  "v_mfma_f32_16x16x32_bf16 v[60:63], v[116:119], v[88:91], v[60:63]\n"
  "s_waitcnt lgkmcnt(6)\n"
  "v_mfma_f32_16x16x32_bf16 v[56:59], v[120:123], v[64:67], v[56:59]\n"
  "v_mfma_f32_16x16x32_bf16 v[60:63], v[124:127], v[64:67], v[60:63]\n"
  "s_waitcnt lgkmcnt(3)\n"
  "v_mfma_f32_16x16x32_bf16 v[56:59], v[36:39], v[68:71], v[56:59]\n"
  "v_mfma_f32_16x16x32_bf16 v[60:63], v[40:43], v[68:71], v[60:63]\n"
  "s_waitcnt lgkmcnt(0)\n"
  "v_mfma_f32_16x16x32_bf16 v[56:59], v[44:47], v[72:75], v[56:59]\n"
  "v_mfma_f32_16x16x32_bf16 v[60:63], v[92:95], v[72:75], v[60:63]\n"
  "ds_read_b128 v[92:95], v8 offset:10240\n"
  "ds_read_b128 v[96:99], v8 offset:11264\n"
  "ds_read_b128 v[100:103], v8 offset:12288\n"
  "ds_read_b128 v[104:107], v8 offset:13312\n"
  "ds_read_b128 v[108:111], v8 offset:14336\n"
  "ds_read_b128 v[112:115], v8 offset:15360\n"
  "v_mov_b64 v[64:65], 0\n"
  "v_mov_b64 v[66:67], 0\n"
  "v_mov_b64 v[68:69], 0\n"
  "v_mov_b64 v[70:71], 0\n"
  "v_mov_b64 v[72:73], 0\n"
  "v_mov_b64 v[74:75], 0\n"
  "v_mov_b64 v[76:77], 0\n"
  "v_mov_b64 v[78:79], 0\n"
  "v_mov_b64 v[80:81], 0\n"
  "v_mov_b64 v[82:83], 0\n"
  "v_mov_b64 v[84:85], 0\n"
  "v_mov_b64 v[86:87], 0\n"
  "v_mov_b64 v[88:89], 0\n"
  "v_mov_b64 v[90:91], 0\n"
  "v_mov_b64 v[120:121], 0\n"
  "v_mov_b64 v[122:123], 0\n"
  "v_mov_b64 v[124:125], 0\n"
  "v_mov_b64 v[126:127], 0\n"
  "v_cvt_pk_bf16_f32 v48, v56, v57\n"
  "v_cvt_pk_bf16_f32 v49, v58, v59\n"
  "v_cvt_pk_bf16_f32 v52, v60, v61\n"
  "v_cvt_pk_bf16_f32 v53, v62, v63\n"
  "v_mov_b64 v[56:57], 0\n"
  "v_mov_b64 v[58:59], 0\n"
  "v_mov_b64 v[60:61], 0\n"
  "v_mov_b64 v[62:63], 0\n"
  "s_sub_u32 s83, s60, 0x3f800000\n"
  "s_sub_u32 s57, 0x3f800000, s83\n"
  "s_sub_u32 s58, 0x3fb8aa3b, s83\n"
  "s_sub_u32 s59, s58, s83\n"
  "s_add_i32 s63, s61, s86\n"
  "v_add_u32 v13, s64, v13\n"
  "v_add_u32 v14, s64, v14\n"
  "s_mov_b32 m0, s52\n"
  "s_mov_b32 s67, s52\n"
  "s_sub_u32 s68, s52, 3072\n"
  "s_add_u32 s69, s52, 512\n"
  "s_add_u32 s52, s52, s64\n"
  "s_sub_u32 s81, s81, s64\n"
  "s_sub_u32 s64, 0, s64\n"
  "s_add_u32 s36, s36, 14336\n"
  "s_addc_u32 s37, s37, 0\n"
  "s_add_u32 s46, s46, 7168\n"
  "s_addc_u32 s47, s47, 0\n"
  "s_add_u32 s56, s56, 1\n"
  "s_mov_b32 s65, 0\n"
  "v_add_u32 v18, s74, v1\n"
  "v_add_u32 v19, s75, v1\n"
  "v_add_u32 v119, s78, v1\n"
  "v_add_u32 v118, s87, v12\n"
  "buffer_load_dwordx4 v[28:31], v12, s[40:43], s44 offen offset:0 sc1\n"
  "buffer_load_dwordx4 v[32:35], v12, s[40:43], s44 offen offset:1024 sc1\n"
  "buffer_load_dwordx4 v[36:39], v12, s[40:43], s70 offen offset:0 sc1\n"
  "buffer_load_dwordx4 v[40:43], v12, s[40:43], s70 offen offset:1024 sc1\n"
  "s_waitcnt vmcnt(3)\n"
  "v_cvt_f32_i32 v44, v28\n"
  "v_cvt_f32_i32 v45, v29\n"
  "v_cvt_f32_i32 v46, v30\n"
  "v_cvt_f32_i32 v47, v31\n"
  "v_min3_i32 v116, v28, v29, v30\n"
  "v_min_i32 v116, v116, v31\n"
  "v_cvt_pk_bf16_f32 v20, v44, v45\n"
  "v_cvt_pk_bf16_f32 v21, v46, v47\n"
  "v_max3_f32 v117, |v44|, |v45|, |v46|\n"
  "v_max_f32 v117, |v47|, v117\n"
  "s_waitcnt vmcnt(2)\n"
  "v_cvt_f32_i32 v44, v32\n"
  "v_cvt_f32_i32 v45, v33\n"
  "v_cvt_f32_i32 v46, v34\n"
  "v_cvt_f32_i32 v47, v35\n"
  "v_min3_i32 v116, v32, v33, v116\n"
  "v_min3_i32 v116, v34, v35, v116\n"
  "v_cvt_pk_bf16_f32 v22, v44, v45\n"
  "v_cvt_pk_bf16_f32 v23, v46, v47\n"
  "ds_write_b128 v1, v[20:23]\n"
  "v_max3_f32 v117, |v44|, |v45|, v117\n"
  "v_max3_f32 v117, |v46|, |v47|, v117\n"
  "s_waitcnt vmcnt(1)\n"
  "v_cvt_f32_i32 v44, v36\n"
  "v_cvt_f32_i32 v45, v37\n"
  "v_cvt_f32_i32 v46, v38\n"
  "v_cvt_f32_i32 v47, v39\n"
  "v_min3_i32 v116, v36, v37, v116\n"
  "v_min3_i32 v116, v38, v39, v116\n"
  "v_cvt_pk_bf16_f32 v24, v44, v45\n"
  "v_cvt_pk_bf16_f32 v25, v46, v47\n"
  "v_max3_f32 v117, |v44|, |v45|, v117\n"
  "v_max3_f32 v117, |v46|, |v47|, v117\n"
  "s_waitcnt vmcnt(0)\n"
  "v_cvt_f32_i32 v44, v40\n"
  "v_cvt_f32_i32 v45, v41\n"
  "v_cvt_f32_i32 v46, v42\n"
  "v_cvt_f32_i32 v47, v43\n"
  "v_min3_i32 v116, v40, v41, v116\n"
  "v_min3_i32 v116, v42, v43, v116\n"
  "v_cvt_pk_bf16_f32 v26, v44, v45\n"
  "v_cvt_pk_bf16_f32 v27, v46, v47\n"
  "v_cmp_gt_i32 vcc, s84, v116\n"
  "ds_write_b128 v18, v[24:27] offset:1024\n"
  "s_cbranch_vccz CH_OK%=\n"
  "CH_POLL%=:\n"
  "s_add_u32 s65, s65, 1\n"
  "s_cmp_gt_u32 s65, %[lim]\n"
  "s_cbranch_scc1 CH_DEAD%=\n"
  "s_cmp_lg_u32 s66, 0\n"
  "s_cbranch_scc1 CH_DEAD%=\n"
  "buffer_load_dwordx4 v[28:31], v12, s[40:43], s44 offen offset:0 sc1\n"
  "buffer_load_dwordx4 v[32:35], v12, s[40:43], s44 offen offset:1024 sc1\n"
  "buffer_load_dwordx4 v[36:39], v12, s[40:43], s70 offen offset:0 sc1\n"
  "buffer_load_dwordx4 v[40:43], v12, s[40:43], s70 offen offset:1024 sc1\n"
  "s_waitcnt vmcnt(3)\n"
  "v_cvt_f32_i32 v44, v28\n"
  "v_cvt_f32_i32 v45, v29\n"
  "v_cvt_f32_i32 v46, v30\n"
  "v_cvt_f32_i32 v47, v31\n"
  "v_min3_i32 v116, v28, v29, v30\n"
  "v_min_i32 v116, v116, v31\n"
  "v_cvt_pk_bf16_f32 v20, v44, v45\n"
  "v_cvt_pk_bf16_f32 v21, v46, v47\n"
  "v_max3_f32 v117, |v44|, |v45|, |v46|\n"
  "v_max_f32 v117, |v47|, v117\n"
  "s_waitcnt vmcnt(2)\n"
  "v_cvt_f32_i32 v44, v32\n"
  "v_cvt_f32_i32 v45, v33\n"
  "v_cvt_f32_i32 v46, v34\n"
  "v_cvt_f32_i32 v47, v35\n"
  "v_min3_i32 v116, v32, v33, v116\n"
  "v_min3_i32 v116, v34, v35, v116\n"
  "v_cvt_pk_bf16_f32 v22, v44, v45\n"
  "v_cvt_pk_bf16_f32 v23, v46, v47\n"
  "ds_write_b128 v1, v[20:23]\n"
  "v_max3_f32 v117, |v44|, |v45|, v117\n"
  "v_max3_f32 v117, |v46|, |v47|, v117\n"
  "s_waitcnt vmcnt(1)\n"
  "v_cvt_f32_i32 v44, v36\n"
  "v_cvt_f32_i32 v45, v37\n"
  "v_cvt_f32_i32 v46, v38\n"
  "v_cvt_f32_i32 v47, v39\n"
  "v_min3_i32 v116, v36, v37, v116\n"
  "v_min3_i32 v116, v38, v39, v116\n"
  "v_cvt_pk_bf16_f32 v24, v44, v45\n"
  "v_cvt_pk_bf16_f32 v25, v46, v47\n"
  "v_max3_f32 v117, |v44|, |v45|, v117\n"
  "v_max3_f32 v117, |v46|, |v47|, v117\n"
  "s_waitcnt vmcnt(0)\n"
  "v_cvt_f32_i32 v44, v40\n"
  "v_cvt_f32_i32 v45, v41\n"
  "v_cvt_f32_i32 v46, v42\n"
  "v_cvt_f32_i32 v47, v43\n"
  "v_min3_i32 v116, v40, v41, v116\n"
  "v_min3_i32 v116, v42, v43, v116\n"
  "v_cvt_pk_bf16_f32 v26, v44, v45\n"
  "v_cvt_pk_bf16_f32 v27, v46, v47\n"
  "v_cmp_gt_i32 vcc, s84, v116\n"
  "ds_write_b128 v18, v[24:27] offset:1024\n"
  "s_cbranch_vccnz CH_POLL%=\n"
  "s_branch CH_OK%=\n"
  "CH_DEAD%=:\n"
  "s_mov_b32 s66, 1\n"
  "CH_OK%=:\n"
  "s_mov_b32 m0, s67\n"
  "s_add_u32 s44, s44, 14336\n"
  "s_add_u32 s70, s70, 14336\n"
  "s_branch CH_LOOP%=\n"
  "CH_TAIL%=:\n"
  "s_waitcnt lgkmcnt(0)\n"
  "v_lshlrev_b32 v19, 16, v18\n"
  "v_and_b32 v18, 0xffff0000, v18\n"
  "v_mul_f32 v19, s57, v19\n"
  "v_mul_f32 v18, s57, v18\n"
  "v_cvt_pk_bf16_f32 v18, v19, v18\n"
  "s_mov_b64 exec, s[48:49]\n"
  "global_store_dword v17, v18, s[46:47] sc1\n"
  "s_mov_b64 exec, -1\n"
  "s_waitcnt vmcnt(0)\n"
  "s_mov_b64 exec, s[76:77]\n"
  "global_store_dword v12, v0, s[54:55] sc1\n"
  "s_mov_b64 exec, -1\n"
  "s_add_u32 s54, s54, 128\n"
  "s_addc_u32 s55, s55, 0\n"
      : "+{v0}"(r0), "+{v1}"(r1), "+{v2}"(r2), "+{v3}"(r3), "+{v4}"(r4), "+{v5}"(r5), "+{v6}"(r6), "+{v7}"(r7), "+{v8}"(r8), "+{v9}"(r9),
        "+{v10}"(r10), "+{v11}"(r11), "+{v12}"(r12), "+{v13}"(r13), "+{v14}"(r14), "+{v15}"(r15), "+{v16}"(r16), "+{v17}"(r17), "+{v18}"(ex), "+{v19}"(em),
        "+{a[0:31]}"(ga[0]), "+{a[32:63]}"(ga[1]), "+{a[64:95]}"(ga[2]), "+{a[96:127]}"(ga[3]), "+{a[128:159]}"(ga[4]), "+{a[160:191]}"(ga[5]),
        "+{a[192:223]}"(ga[6]), "+{a[224:255]}"(ga[7]), "+{v[128:159]}"(gv[0]), "+{v[160:191]}"(gv[1]), "+{v[192:223]}"(gv[2]), "+{v[224:255]}"(gv[3]), "+{v127}"(dm)
      : [ws] "s"(ws), [h] "s"(h), [c] "s"(c), [w] "s"(w), [lim] "n"(200000)
      :
"v20", "v21", "v22", "v23", "v24", "v25", "v26", "v27", "v28", "v29", "v30", "v31", "v32", "v33", "v34", "v35", "v36", "v37", "v38", "v39", "v40", "v41", "v42", "v43", "v44", "v45", "v46", "v47", "v48", "v49", "v50", "v51", "v52", "v53", "v54", "v55", "v56", "v57", "v58", "v59", "v60", "v61", "v62", "v63", "v64", "v65", "v66", "v67", "v68", "v69", "v70", "v71", "v72", "v73", "v74", "v75", "v76", "v77", "v78", "v79", "v80", "v81", "v82", "v83", "v84", "v85", "v86", "v87", "v88", "v89", "v90", "v91", "v92", "v93", "v94", "v95", "v96", "v97", "v98", "v99", "v100", "v101", "v102", "v103", "v104", "v105", "v106", "v107", "v108", "v109", "v110", "v111", "v112", "v113", "v114", "v115", "v116", "v117", "v118", "v119", "v120", "v121", "v122", "v123", "v124", "v125", "v126", "s36", "s37", "s38", "s39", "s40", "s41", "s42", "s43", "s44", "s45", "s46", "s47", "s48", "s49", "s50", "s51", "s52", "s53", "s54", "s55", "s56", "s57", "s58", "s59", "s60", "s61", "s62", "s63", "s64", "s65", "s66", "s67", "s68", "s69", "s70", "s71", "s72", "s73", "s74", "s75", "s76", "s77", "s78", "s79", "s80", "s81", "s82", "s83", "s84", "s85", "s86", "s87", "s88", "s89", "s90", "s91", "s92", "s93", "s94", "s95", "s96", "s97", "s98", "s99", "s100", "s101", "vcc", "scc", "memory"
  );
}
#define SLP 3
constexpr unsigned F_P = (unsigned)OFF_FLG, F_H = F_P + 8192, F_A = F_P + 24576, F_C = F_P + 131072;
constexpr unsigned O_REC = (unsigned)OFF_REC, O_HF = O_REC + 1835008u, O_ACT = O_HF + 1835008u, O_P = O_ACT + 4 * 1835008u;
typedef __amdgpu_buffer_rsrc_t rsrc_t;
DI u32x4 ld16(rsrc_t r, unsigned o) { return __builtin_amdgcn_raw_buffer_load_b128(r, o, 0, 16); }
DI u32x2 ld8(rsrc_t r, unsigned o) { return __builtin_amdgcn_raw_buffer_load_b64(r, o, 0, 16); }
DI unsigned ld4(rsrc_t r, unsigned o) { return __builtin_amdgcn_raw_buffer_load_b32(r, o, 0, 16); }
DI void st16(rsrc_t r, unsigned o, u32x4 v) { __builtin_amdgcn_raw_buffer_store_b128(v, r, o, 0, 16); }
DI void st8(rsrc_t r, unsigned o, u32x2 v) { __builtin_amdgcn_raw_buffer_store_b64(v, r, o, 0, 16); }
DI void st4(rsrc_t r, unsigned o, unsigned v) { __builtin_amdgcn_raw_buffer_store_b32(v, r, o, 0, 16); }
DI void vm0() { asm volatile("s_waitcnt vmcnt(0)" ::: "memory"); }
DI void vm_barrier() { asm volatile("s_waitcnt vmcnt(0) lgkmcnt(0)\n\ts_barrier" ::: "memory"); }
#define PSCAP 10
struct Pace { int budget, ps, n; };
#define FASTU 30
template <unsigned SET> DI void wait_flags(rsrc_t r, unsigned o, int n, int lane, int w, Pace& p) {
  if (w == 0) {
    const unsigned a = o + (lane < n ? lane : 0) * 4;
    const bool fast = p.n >= FASTU;
    if (!fast) for (int i = p.ps; i > 0; --i) __builtin_amdgcn_s_sleep(1);
    int it = 0;
    for (; p.budget > 0; --p.budget, ++it) {
      unsigned v;
      asm volatile("buffer_load_dword %0, %1, %2, 0 offen sc1\n\ts_waitcnt vmcnt(0)" : "=v"(v) : "v"(a), "s"(r) : "memory");
      if (__builtin_amdgcn_ballot_w64(v != SET) == 0ull) break;
      if (fast) __builtin_amdgcn_s_sleep(2); else __builtin_amdgcn_s_sleep(SLP);
    }
    p.ps = min((p.ps + (12 + SLP) * it) * 3 >> 2, PSCAP);
  }
  asm volatile("s_barrier" ::: "memory");
}
template <int V> struct IC { static constexpr int value = V; };
#define SB() __builtin_amdgcn_sched_barrier(0)
template <typename T> DI void touch(T& v) { asm volatile("" : "+v"(v)); }
template <typename T> DI void toucha(T& v) { asm volatile("" : "+a"(v)); }
DI void set_flag(rsrc_t r, unsigned o, int lane) { vm0(); if (lane == 0) st4(r, o, 1u); }
DI bf16x8 bc8(u32x4 v) { return __builtin_bit_cast(bf16x8, v); }
constexpr unsigned L_R0 = 65536, L_A6 = 86016;
DI int role_id() {
  const int r = (blockIdx.x & 7) * 32 + (blockIdx.x >> 3);
  if (r < 128) return r;
  const int x = (r - 128) >> 5, i = r & 31;
  return i < 4 ? 128 + 4 * x + i : i < 18 ? 140 + 14 * x + i : i < 25 ? 182 + 7 * x + i : 203 + 7 * x + i;
}
#define BID role_id()

DI void y_role(const int h, const int par, unsigned char* ws) {
  const int tid = threadIdx.x, lane = tid & 63, w = __builtin_amdgcn_readfirstlane(tid >> 6);
  const int n16 = lane & 15, g = lane >> 4;
  Pace pc = {1 << 18, 0, 0};
  const rsrc_t rs = __builtin_amdgcn_make_buffer_rsrc(ws, 0, 0x8000000, 0x00020000);
  dma_kb<7>(ws + OFF_XF, lane * 16 + w * 7168 + par * 28672, w * 7168);
  dma_kb<14>(ws + OFF_AF + (size_t)h * MATF + (size_t)w * 100352 + 86016, lane * 16, L_A6 + w * 14336);
  bf16x8 A[6][14];
  {
    const unsigned char* as = ws + OFF_AF + (size_t)h * MATF + (size_t)w * 100352 + lane * 16;
#pragma unroll
    for (int t = 0; t < 6; ++t)
#pragma unroll
      for (int k = 0; k < 14; ++k) A[t][k] = *(const GAS bf16x8*)(as + (t * 14 + k) * 1024);
  }
#pragma unroll
  for (int i = 0; i < 84; ++i) { if (i < 48) toucha(A[i / 14][i % 14]); else touch(A[i / 14][i % 14]); }
  const unsigned a6 = L_A6 + w * 14336 + lane * 16;
  const int bsh = 12 + (w >> 1);
  const unsigned mrow = ((n16 >> 2) + 4 * (n16 & 1)) * 32 + (g >> 1) * 256 + (g & 1) * 16;
  vm_barrier();
  for (int u = 0; u < 32; ++u) {
    const int s = 2 * u + par;
    pc.n = u;
    const unsigned xb = (u & 1) * 32768 + lane * 16;
    if (u < 31) dma_kb<7>(ws + OFF_XF, lane * 16 + w * 7168 + (s + 2) * 28672, ((u & 1) ^ 1) * 32768 + w * 7168);
    f32x4 acc[7][2];
#pragma unroll
    for (int t = 0; t < 7; ++t) { acc[t][0] = f32x4{0, 0, 0, 0}; acc[t][1] = f32x4{0, 0, 0, 0}; }
    bf16x8 xa[2][3];
    unsigned fl;
    xa[0][0] = lds_ld<bf16x8>(xb); xa[0][1] = lds_ld<bf16x8>(xb + 14336); xa[0][2] = lds_ld<bf16x8>(a6);
#pragma unroll
    for (int k = 0; k < 14; ++k) {
      if (k < 13) { xa[(k + 1) & 1][0] = lds_ld<bf16x8>(xb + (k + 1) * 1024); xa[(k + 1) & 1][1] = lds_ld<bf16x8>(xb + 14336 + (k + 1) * 1024); xa[(k + 1) & 1][2] = lds_ld<bf16x8>(a6 + (k + 1) * 1024); }
      if (k == 3 && w < 2 && u > 0) {
        if (u < 31) asm volatile("s_waitcnt vmcnt(7)" ::: "memory"); else vm0();
        if (lane == 0) st4(rs, F_P + (((s - 2) * 16 + h) * 2 + w) * 4, 1u);
      }
      if (k == 10) asm volatile("buffer_load_dword %0, %1, %2, 0 offen sc1" : "=v"(fl) : "v"(F_C + s * 128 + (lane < 28 ? lane : 0) * 4), "s"(rs) : "memory");
      SB();
#pragma unroll
      for (int t = 0; t < 7; ++t) { const bf16x8 af = t < 6 ? A[t < 6 ? t : 0][k] : xa[k & 1][2]; acc[t][0] = mfma32(af, xa[k & 1][0], acc[t][0]); acc[t][1] = mfma32(af, xa[k & 1][1], acc[t][1]); }
      SB();
    }
    u32x4 yf[4][2];
#pragma unroll
    for (int tt = 0; tt < 2; ++tt) {
#pragma unroll
      for (int b = 0; b < 3; ++b) {
        const f32x4 p0 = acc[2 * b][tt], p1 = acc[2 * b + 1][tt];
        yf[b][tt] = u32x4{pk2(p0[0], p0[1]), pk2(p1[0], p1[1]), pk2(p0[2], p0[3]), pk2(p1[2], p1[3])};
      }
      const unsigned d0 = pk2(acc[6][tt][0], acc[6][tt][1]), d1 = pk2(acc[6][tt][2], acc[6][tt][3]);
      yf[3][tt] = (w & 1) ? u32x4{0, d0, 0, d1} : u32x4{d0, 0, d1, 0};
    }
    asm volatile("s_waitcnt vmcnt(0)" : "+v"(fl) : : "memory");
    const bool early = __builtin_amdgcn_ballot_w64(fl != 0x4444u) == 0ull;
    const unsigned mb = O_REC + s * 7168 + mrow;
    u32x4 mk[4];
    if (early) {
#pragma unroll
      for (int b = 0; b < 4; ++b) mk[b] = ld16(rs, mb + (b < 3 ? 3 * w + b : bsh) * 512);
    }
    SB();
    f32x4 s00 = {0, 0, 0, 0}, s01 = {0, 0, 0, 0}, s11 = {0, 0, 0, 0};
#pragma unroll
    for (int b = 0; b < 4; ++b) {
      const unsigned ko = (b < 3 ? 3 * w + b : bsh) * 1024;
      const bf16x8 k0 = lds_ld<bf16x8>(xb + ko), k1 = lds_ld<bf16x8>(xb + 14336 + ko);
      s00 = mfma32(k0, bc8(yf[b][0]), s00); s01 = mfma32(k0, bc8(yf[b][1]), s01); s11 = mfma32(k1, bc8(yf[b][1]), s11);
    }
    lds_st<f32x4>(L_R0 + w * 3072 + lane * 16, s00); lds_st<f32x4>(L_R0 + w * 3072 + 1024 + lane * 16, s01); lds_st<f32x4>(L_R0 + w * 3072 + 2048 + lane * 16, s11);
    if (tid == 0) lds_st<unsigned>(L_R0 + 16384, early ? 1u : 0u);
    lds_barrier();
    const bool go = __builtin_amdgcn_readfirstlane(lds_ld<unsigned>(L_R0 + 16384)) != 0u;
    f32x4 e0 = {0, 0, 0, 0}, e1 = {0, 0, 0, 0};
    if (w < 2) {
#pragma unroll
      for (int q = 0; q < 4; ++q) { e0 += lds_ld<f32x4>(L_R0 + q * 3072 + w * 1024 + lane * 16); e1 += lds_ld<f32x4>(L_R0 + q * 3072 + 2048 + lane * 16); }
#pragma unroll
      for (int i = 0; i < 4; ++i) {
        const bool ok = 4 * g + i <= n16;
        if (w == 0) { e0[i] = ok ? e0[i] : -1e30f; e1[i] = -1e30f; } else e1[i] = ok ? e1[i] : -1e30f;
      }
    }
    if (!go) wait_flags<0x4444u>(rs, F_C + s * 128, 28, lane, w, pc);
    {
      if (!early) {
#pragma unroll
        for (int b = 0; b < 4; ++b) mk[b] = ld16(rs, mb + (b < 3 ? 3 * w + b : bsh) * 512);
      }
      f32x4 m0 = {0, 0, 0, 0}, m1 = {0, 0, 0, 0};
#pragma unroll
      for (int b = 0; b < 4; ++b) { m0 = mfma32(bc8(mk[b]), bc8(yf[b][0]), m0); m1 = mfma32(bc8(mk[b]), bc8(yf[b][1]), m1); }
      lds_st<f32x4>(L_R0 + 12288 + w * 1024 + lane * 16, f32x4{m0[0], m0[1], m1[0], m1[1]});
    }
    vm_barrier();
    if (w < 2) {
      f32x2 em = {0, 0};
#pragma unroll
      for (int q = 0; q < 4; ++q) em += lds_ld<f32x2>(L_R0 + 12288 + q * 1024 + lane * 16 + w * 8);
      float mx = fmaxf(fmaxf(fmaxf(e0[0], e0[1]), fmaxf(e0[2], e0[3])), fmaxf(fmaxf(e1[0], e1[1]), fmaxf(e1[2], e1[3])));
      mx = xor32_max(xor16_max(fmaxf(mx, fmaxf(em[0], em[1]))));
      const float L2E = 1.44269504f, mo = mx * L2E;
      float l = 0.f;
#pragma unroll
      for (int i = 0; i < 4; ++i) {
        e0[i] = __builtin_amdgcn_exp2f(e0[i] * L2E - mo); e1[i] = __builtin_amdgcn_exp2f(e1[i] * L2E - mo); l += e0[i] + e1[i];
      }
      em[0] = __builtin_amdgcn_exp2f(em[0] * L2E - mo); em[1] = __builtin_amdgcn_exp2f(em[1] * L2E - mo);
      l = xor32_add(xor16_add(l + em[0] + em[1]));
      const float r = __builtin_amdgcn_rcpf(l);
      const unsigned pb = O_P + (s * 16 + h) * 2560;
      st16(rs, pb + w * 1024 + lane * 16, u32x4{pk2(e0[0] * r, e0[1] * r), pk2(e0[2] * r, e0[3] * r), pk2(e1[0] * r, e1[1] * r), pk2(e1[2] * r, e1[3] * r)});
      st4(rs, pb + 2048 + w * 256 + lane * 4, pk2(em[0] * r, em[1] * r));
      if (u == 31) set_flag(rs, F_P + ((s * 16 + h) * 2 + w) * 4, lane);
    }
  }
}

DI void z_role(const int T, const int par, const bf16_t* __restrict__ x, unsigned char* ws) {
  const int tid = threadIdx.x, lane = tid & 63, w = __builtin_amdgcn_readfirstlane(tid >> 6);
  const int n16 = lane & 15, g = lane >> 4;
  Pace pc = {1 << 18, 0, 0}, pm = {1 << 18, 0, 0};
  const rsrc_t rs = __builtin_amdgcn_make_buffer_rsrc(ws, 0, 0x8000000, 0x00020000);
  unsigned pre[8];
#pragma unroll
  for (int i = 0; i < 8; ++i)
    asm volatile("buffer_load_dword %0, %1, %2, 0 offen sc1" : "=v"(pre[i]) : "v"(O_REC + ((T * 2 + par) * 256 + tid) * 128 + i * 1835008u), "s"(rs) : "memory");
  dma_kb<7>(ws + OFF_XF, lane * 16 + w * 7168 + par * 28672, w * 7168);
  bf16x8 B[4][14];
#pragma unroll
  for (int q = 0; q < 4; ++q)
#pragma unroll
    for (int k = 0; k < 14; ++k) B[q][k] = *(const GAS bf16x8*)(ws + OFF_BF + (size_t)(4 * w + q) * MATF + (size_t)(T * 14 + k) * 1024 + lane * 16);
#pragma unroll
  for (int i = 0; i < 56; ++i) toucha(B[i / 14][i % 14]);
#pragma unroll
  for (int i = 0; i < 8; ++i) asm volatile("s_waitcnt vmcnt(0)" : "+v"(pre[i]) : : "memory");
  const unsigned mrow = ((n16 >> 2) + 4 * (n16 & 1)) * 32 + (g >> 1) * 256 + (g & 1) * 16;
  const int c0 = 16 * T + 8 * (g & 1) + 2 * (g >> 1);
  const unsigned ho = (T >> 1) * 1024 + ((2 * (T & 1) + (g & 1)) * 16 + n16) * 16 + 4 * (g >> 1);
  dma_kb<7>(ws + OFF_XF, lane * 16 + w * 7168 + (par + 2) * 28672, 32768 + w * 7168);
  vm_barrier();
  f32x4 z[4][2];
  u32x4 zf[4];
  auto zx = [&](unsigned xb, auto K0c) {
    constexpr int K0 = decltype(K0c)::value;
    bf16x8 xa[3][2];
#pragma unroll
    for (int k = 0; k < 2; ++k) { xa[k][0] = lds_ld<bf16x8>(xb + (K0 + k) * 1024); xa[k][1] = lds_ld<bf16x8>(xb + 14336 + (K0 + k) * 1024); }
    if (K0 == 0) {
#pragma unroll
      for (int q = 0; q < 4; ++q) { z[q][0] = f32x4{0, 0, 0, 0}; z[q][1] = f32x4{0, 0, 0, 0}; }
    }
#pragma unroll
    for (int k = 0; k < 7; ++k) {
      if (k < 5) { xa[(k + 2) % 3][0] = lds_ld<bf16x8>(xb + (K0 + k + 2) * 1024); xa[(k + 2) % 3][1] = lds_ld<bf16x8>(xb + 14336 + (K0 + k + 2) * 1024); }
      SB();
#pragma unroll
      for (int q = 0; q < 4; ++q) { z[q][0] = mfma32(xa[k % 3][0], B[q][K0 + k], z[q][0]); z[q][1] = mfma32(xa[k % 3][1], B[q][K0 + k], z[q][1]); }
      SB();
    }
  };
  auto zpack = [&]() {
#pragma unroll
    for (int q = 0; q < 4; ++q) zf[q] = u32x4{pk2(z[q][0][0], z[q][0][1]), pk2(z[q][0][2], z[q][0][3]), pk2(z[q][1][0], z[q][1][1]), pk2(z[q][1][2], z[q][1][3])};
  };
  zx(lane * 16, IC<0>{}); zx(lane * 16, IC<7>{}); zpack();
  bool early = false;
  u32x4 mk[14];
  for (int u = 0; u < 32; ++u) {
    const int s = 2 * u + par;
    pc.n = u;
    const unsigned xn = ((u & 1) ^ 1) * 32768 + lane * 16;
    unsigned xr0 = 0, xr1 = 0;
    if (w < 2) { const bf16_t* xp = x + (size_t)(s * 32 + w * 16 + n16) * DM + c0; xr0 = *(const unsigned*)xp; xr1 = *(const unsigned*)(xp + 4); }
    if (!early) {
      pm.n = u;
      wait_flags<0x4444u>(rs, F_C + s * 128, 28, lane, w, pm);
#pragma unroll
      for (int k = 0; k < 14; ++k) mk[k] = ld16(rs, O_REC + s * 7168 + mrow + k * 512);
    }
    unsigned zm[4];
    {
      SB();
      f32x4 m[4];
#pragma unroll
      for (int q = 0; q < 4; ++q) m[q] = f32x4{0, 0, 0, 0};
#pragma unroll
      for (int k = 0; k < 14; ++k)
#pragma unroll
        for (int q = 0; q < 4; ++q) m[q] = mfma32(bc8(mk[k]), B[q][k], m[q]);
#pragma unroll
      for (int q = 0; q < 4; ++q) zm[q] = pk2(m[q][0], m[q][1]);
    }
    SB();
    wait_flags<1u>(rs, F_P + s * 128, 32, lane, w, pc);
    u32x4 p0[4], p1[4]; unsigned q0[4], q1[4];
#pragma unroll
    for (int q = 0; q < 4; ++q) {
      const unsigned pb = O_P + (s * 16 + 4 * w + q) * 2560;
      p0[q] = ld16(rs, pb + lane * 16); p1[q] = ld16(rs, pb + 1024 + lane * 16);
      q0[q] = ld4(rs, pb + 2048 + lane * 4); q1[q] = ld4(rs, pb + 2304 + lane * 4);
    }
    if (u < 30) dma_kb<7>(ws + OFF_XF, lane * 16 + w * 7168 + (s + 4) * 28672, (u & 1) * 32768 + w * 7168);
    unsigned fl = 0;
    if (w == 3 && u < 31) asm volatile("buffer_load_dword %0, %1, %2, 0 offen sc1" : "=v"(fl) : "v"(F_C + (s + 2) * 128 + (lane < 28 ? lane : 0) * 4), "s"(rs) : "memory");
    SB();
    if (u < 31) zx(xn, IC<0>{});
    f32x4 o0 = {0, 0, 0, 0}, o1 = {0, 0, 0, 0};
#pragma unroll
    for (int q = 0; q < 4; ++q) {
      o0 = mfma32(bc8(zf[q]), bc8(p0[q]), o0); o1 = mfma32(bc8(zf[q]), bc8(p1[q]), o1);
      o0 = mfma32(bc8(u32x4{zm[q], 0, 0, 0}), bc8(u32x4{q0[q], 0, 0, 0}), o0);
      o1 = mfma32(bc8(u32x4{zm[q], 0, 0, 0}), bc8(u32x4{q1[q], 0, 0, 0}), o1);
    }
    lds_st<f32x4>(L_R0 + w * 2048 + lane * 16, o0); lds_st<f32x4>(L_R0 + w * 2048 + 1024 + lane * 16, o1);
    asm volatile("s_waitcnt vmcnt(0)" : "+v"(fl) : : "memory");
    if (w == 3) { const unsigned e = __builtin_amdgcn_ballot_w64(fl != 0x4444u) == 0ull; if (lane == 0) lds_st<unsigned>(L_R0 + 8192 + (u & 1) * 4, e); }
    vm_barrier();
    early = __builtin_amdgcn_readfirstlane(lds_ld<unsigned>(L_R0 + 8192 + (u & 1) * 4)) != 0u;
    if (w < 2) {
      f32x4 a = {0, 0, 0, 0};
#pragma unroll
      for (int q = 0; q < 4; ++q) a += lds_ld<f32x4>(L_R0 + q * 2048 + w * 1024 + lane * 16);
      const unsigned a0 = pk2(a[0], a[1]), a1 = pk2(a[2], a[3]);
      const unsigned hb = O_HF + (s * 2 + w) * 14336 + ho;
      st4(rs, hb, pk2(bflo(xr0) + bflo(a0), bfhi(xr0) + bfhi(a0)));
      st4(rs, hb + 8, pk2(bflo(xr1) + bflo(a1), bfhi(xr1) + bfhi(a1)));
    }
    if (early) {
#pragma unroll
      for (int k = 0; k < 14; ++k) mk[k] = ld16(rs, O_REC + (s + 2) * 7168 + mrow + k * 512);
    }
    SB();
    if (u < 31) { zx(xn, IC<7>{}); zpack(); }
    if (w < 2) set_flag(rs, F_H + (s * 64 + T * 2 + w) * 4, lane);
  }
}
DI int wsw(int row) { return ((row >> 1) & 1) | (((row >> 3) & 1) << 1); }
template <int GAP> DI void load_wfrags(const unsigned char* p, ptrdiff_t odd, size_t rstep, int w, int lane, bf16x8 (&F)[14][4], int k0) {
  const unsigned lb = w * 36864;
#pragma unroll
  for (int i = 0; i < 28; ++i) {
    unsigned sv;
    asm volatile("s_mov_b32 %0, m0\n\ts_mov_b32 m0, %2\n\ts_nop 3\n\tglobal_load_lds_dwordx4 %1, off\n\ts_mov_b32 m0, %0"
                 : "=&s"(sv) : "v"(p + i * rstep + ((i & 1) ? odd : 0)), "s"(lb + i * 1024u) : "memory");
  }
  vm0();
  const int n16 = lane & 15, g = lane >> 4, row = (GAP == 4 ? 8 : 4) * g + (n16 >> 2);
#pragma unroll
  for (int t = 0; t < 4; ++t) {
    const unsigned a = lb + row * 128 + ((t ^ wsw(row)) * 32) + (n16 & 3) * 8;
#pragma unroll
    for (int k = 0; k < 7; ++k) {
      const u32x2 lo = lds_tr(a + k * 4096), hi = lds_tr(a + k * 4096 + GAP * 128);
      F[k0 + k][t] = bc8(u32x4{lo[0], lo[1], hi[0], hi[1]});
      toucha(F[k0 + k][t]);
    }
  }
  asm volatile("s_waitcnt lgkmcnt(0)" ::: "memory");
}
DI void g_role(const int j, const int par, const bf16_t* __restrict__ Wg, const bf16_t* __restrict__ Wu, unsigned char* ws) {
  const int tid = threadIdx.x, lane = tid & 63, w = __builtin_amdgcn_readfirstlane(tid >> 6);
  const int n16 = lane & 15, g = lane >> 4;
  Pace pc = {1 << 18, 0, 0};
  const rsrc_t rs = __builtin_amdgcn_make_buffer_rsrc(ws, 0, 0x8000000, 0x00020000);
  bf16x8 W[14][4];
  {
    const int pc8 = (lane & 7) ^ (wsw(lane >> 3) << 1);
    const bf16_t* src = (pc8 < 4 ? Wg : Wu) + (size_t)(lane >> 3) * FF + 128 * j + 32 * w + (pc8 & 3) * 8;
    load_wfrags<4>((const unsigned char*)src, ((const unsigned char*)(pc8 < 4 ? Wu : Wg) - (const unsigned char*)(pc8 < 4 ? Wg : Wu)), (size_t)16 * FF, w, lane, W, 0);
    load_wfrags<4>((const unsigned char*)(src + (size_t)224 * FF), ((const unsigned char*)(pc8 < 4 ? Wu : Wg) - (const unsigned char*)(pc8 < 4 ? Wg : Wu)), (size_t)16 * FF, w, lane, W, 7);
  }
  lds_barrier();
  const unsigned f0 = w * 7168 + lane * 16;
  for (int u = 0; u < 32; ++u) {
    const int s = 2 * u + par;
    pc.n = u;
    wait_flags<1u>(rs, F_H + s * 256, 56, lane, w, pc);
    u32x4 v[7];
#pragma unroll
    for (int k = 0; k < 7; ++k) v[k] = ld16(rs, O_HF + s * 28672 + f0 + k * 1024);
    SB();
    f32x2 s2 = {0, 0};
#pragma unroll
    for (int i = 0; i < 28; ++i) { const unsigned d = v[i >> 2][i & 3]; s2 += f32x2{bflo(d), bfhi(d)}; }
    const float ml = xor32_add(xor16_add(s2[0] + s2[1])) * (1.0f / 224);
    f32x2 q2 = {0, 0};
#pragma unroll
    for (int i = 0; i < 28; ++i) { const unsigned d = v[i >> 2][i & 3]; const f32x2 e = f32x2{bflo(d), bfhi(d)} - ml; q2 += e * e; }
    const float ql = xor32_add(xor16_add(q2[0] + q2[1]));
    if (g == 0) lds_st<f32x2>(L_R0 + (w >> 1) * 256 + n16 * 16 + (w & 1) * 8, f32x2{ml, ql});
    lds_barrier();
    const f32x4 m4 = lds_ld<f32x4>(L_R0 + (w >> 1) * 256 + n16 * 16);
    const float mean = 0.5f * (m4[0] + m4[2]), dm = 0.5f * (m4[0] - m4[2]);
    const float var = (m4[1] + m4[3] + 448.0f * dm * dm) * (1.0f / DM);
    const float mb = rbf(mean), rd = 1.0f / rbf(sqrtf(rbf(rbf(var) + 1e-5f)));
#pragma unroll
    for (int k = 0; k < 7; ++k) {
      u32x4 o;
#pragma unroll
      for (int i = 0; i < 4; ++i) { const unsigned d = v[k][i]; const unsigned r = pk2(bflo(d) - mb, bfhi(d) - mb); o[i] = pk2(bflo(r) * rd, bfhi(r) * rd); }
      lds_st<u32x4>(f0 + k * 1024, o);
    }
    lds_barrier();
    f32x4 ag[2][2], au[2][2];
#pragma unroll
    for (int i = 0; i < 4; ++i) { ag[i >> 1][i & 1] = f32x4{0, 0, 0, 0}; au[i >> 1][i & 1] = f32x4{0, 0, 0, 0}; }
    bf16x8 xa[4][2];
#pragma unroll
    for (int k = 0; k < 3; ++k) { xa[k][0] = lds_ld<bf16x8>(lane * 16 + k * 1024); xa[k][1] = lds_ld<bf16x8>(lane * 16 + 14336 + k * 1024); }
#pragma unroll
    for (int k = 0; k < 14; ++k) {
      if (k < 11) { xa[(k + 3) & 3][0] = lds_ld<bf16x8>(lane * 16 + (k + 3) * 1024); xa[(k + 3) & 3][1] = lds_ld<bf16x8>(lane * 16 + 14336 + (k + 3) * 1024); }
      SB();
#pragma unroll
      for (int i = 0; i < 4; ++i) {
        ag[i >> 1][i & 1] = mfma32(W[k][i & 1], xa[k & 3][i >> 1], ag[i >> 1][i & 1]);
        au[i >> 1][i & 1] = mfma32(W[k][2 + (i & 1)], xa[k & 3][i >> 1], au[i >> 1][i & 1]);
      }
      SB();
    }
#pragma unroll
    for (int tt = 0; tt < 2; ++tt) {
      u32x4 r;
#pragma unroll
      for (int i = 0; i < 4; ++i) {
        const f32x4 a = ag[tt][i >> 1], b = au[tt][i >> 1];
        const unsigned gg = pk2(a[2 * (i & 1)], a[2 * (i & 1) + 1]), uu = pk2(b[2 * (i & 1)], b[2 * (i & 1) + 1]);
        r[i] = pk2(fmaxf(bflo(gg), 0.f) * bflo(uu), fmaxf(bfhi(gg), 0.f) * bfhi(uu));
      }
      st16(rs, O_ACT + ((s * 2 + tt) * 56 + 4 * j + w) * 1024 + lane * 16, r);
    }
    set_flag(rs, F_A + (s * 64 + j * 4 + w) * 4, lane);
  }
}
DI void d_role(const int cg, const int tt, const int par, const bf16_t* __restrict__ Wd, bf16_t* __restrict__ out, unsigned char* ws) {
  const int tid = threadIdx.x, lane = tid & 63, w = __builtin_amdgcn_readfirstlane(tid >> 6);
  const int n16 = lane & 15, g = lane >> 4;
  Pace pc = {1 << 18, 0, 0};
  const rsrc_t rs = __builtin_amdgcn_make_buffer_rsrc(ws, 0, 0x8000000, 0x00020000);
  bf16x8 D[14][4];
  {
    const int pc8 = (lane & 7) ^ (wsw(lane >> 3) << 1);
    const bf16_t* src = Wd + (size_t)(448 * w + (lane >> 3)) * DM + 64 * cg + pc8 * 8;
    load_wfrags<16>((const unsigned char*)src, pc8 < 4 ? 64 : -64, (size_t)16 * DM, w, lane, D, 0);
    load_wfrags<16>((const unsigned char*)(src + (size_t)224 * DM), pc8 < 4 ? 64 : -64, (size_t)16 * DM, w, lane, D, 7);
  }
  lds_barrier();
  for (int u = 0; u < 32; ++u) {
    const int s = 2 * u + par;
    pc.n = u;
    wait_flags<1u>(rs, F_A + s * 256, 56, lane, w, pc);
    u32x4 v[14];
#pragma unroll
    for (int k = 0; k < 14; ++k) v[k] = ld16(rs, O_ACT + ((s * 2 + tt) * 56 + 14 * w + k) * 1024 + lane * 16);
    const u32x2 hv = ld8(rs, O_HF + ((s * 2 + tt) * 14 + 2 * cg + (w >> 1)) * 1024 + ((2 * (w & 1) + (g >> 1)) * 16 + n16) * 16 + (g & 1) * 8);
    SB();
    f32x4 o[4];
#pragma unroll
    for (int ct = 0; ct < 4; ++ct) o[ct] = f32x4{0, 0, 0, 0};
#pragma unroll
    for (int k = 0; k < 14; ++k)
#pragma unroll
      for (int ct = 0; ct < 4; ++ct) o[ct] = mfma32(D[k][ct], bc8(v[k]), o[ct]);
    const unsigned rb = L_R0 + (u & 1) * 16384;
#pragma unroll
    for (int ct = 0; ct < 4; ++ct) lds_st<f32x4>(rb + w * 4096 + ct * 1024 + lane * 16, o[ct]);
    lds_barrier();
    f32x4 a = {0, 0, 0, 0};
#pragma unroll
    for (int q = 0; q < 4; ++q) a += lds_ld<f32x4>(rb + q * 4096 + w * 1024 + lane * 16);
    const unsigned a0 = pk2(a[0], a[1]), a1 = pk2(a[2], a[3]);
    *(u32x2*)(out + (size_t)(s * 32 + tt * 16 + n16) * DM + 64 * cg + 16 * w + 4 * g) =
        u32x2{pk2(bflo(hv[0]) + bflo(a0), bfhi(hv[0]) + bfhi(a0)), pk2(bflo(hv[1]) + bflo(a1), bfhi(hv[1]) + bfhi(a1))};
  }
}

__global__ void __launch_bounds__(256, 1) k_main(const bf16_t* __restrict__ x, const bf16_t* __restrict__ mem0, const bf16_t* __restrict__ Wg,
                                                 const bf16_t* __restrict__ Wu, const bf16_t* __restrict__ Wd, bf16_t* __restrict__ out,
                                                 unsigned char* ws) {
  __shared__ __attribute__((aligned(16))) unsigned char sm[L_CH_END];
  if ((unsigned)(uintptr_t)(LAS unsigned char*)sm != 0u) return;
  asm volatile("" : "+s"(ws) : : "memory");
  const int b = role_id();
  if (b < 112) {
    chain_role(b / 7, b % 7, mem0, ws);
  } else if (b < 144) y_role((b - 112) & 15, (b - 112) >> 4, ws);
  else if (b < 200) z_role((b - 144) >> 1, b & 1, x, ws);
  else if (b < 228) g_role((b - 200) >> 1, b & 1, Wg, Wu, ws);
  else d_role((b - 228) >> 2, (b >> 1) & 1, b & 1, Wd, out, ws);
}

extern "C" void kernel_launch(void* const* d_in, const int* in_sizes, int n_in, void* d_out, int out_size, void* d_ws, size_t ws_size, hipStream_t stream) {
  unsigned char* ws = (unsigned char*)d_ws;
  k_prologue<<<256, 512, 0, stream>>>((const bf16_t*)d_in[0], (const bf16_t*)d_in[2], (const bf16_t*)d_in[3], (const bf16_t*)d_in[4], (const bf16_t*)d_in[5], ws);
  k_main<<<NB, 256, 0, stream>>>((const bf16_t*)d_in[0], (const bf16_t*)d_in[1], (const bf16_t*)d_in[8], (const bf16_t*)d_in[9], (const bf16_t*)d_in[10], (bf16_t*)d_out, ws);
}
```
